```python
import torch

def _softmax(x, axis=-1):
    return torch.softmax(x, dim=axis)
def _gelu(x):
    return torch.nn.functional.gelu(x, approximate="tanh")
def _silu(x):
    return torch.nn.functional.silu(x)
def _sigmoid(x):
    return torch.sigmoid(x)
def _layernorm(x, axis=-1, eps=1e-5):
    m = x.mean(dim=axis, keepdim=True)
    v = x.var(dim=axis, keepdim=True, unbiased=False)
    return (x - m) / torch.sqrt(v + eps)
def _scatter_add(tgt, idx, src, axis=0):
    return tgt.clone().index_add_(axis, idx, src)
def _segment_sum(src, seg, n):
    out = torch.zeros((n,) + src.shape[1:], dtype=src.dtype)
    return out.index_add_(0, seg, src)
def _pad(x, pw):
    flat = tuple(v for pair in reversed(pw) for v in pair)
    return torch.nn.functional.pad(x, flat)

kernel_name = 'sequential_preLN__sliding_local_dilated__glu_gated_geglu__prompt'

def setup_inputs():

    _w = (lambda a: (lambda t: t.to(torch.bfloat16) if t.is_floating_point() else t)(torch.as_tensor(a)))
    def _A_setup():
        B, S, D = 1, 2048, 512
        return {
            "x": _w((lambda *s: torch.randn(*s, dtype=torch.bfloat16) * 0.05)(B, S, D)),
            "wqkv": _w((lambda *s: torch.randn(*s, dtype=torch.bfloat16) * 0.05)(D, 3 * D)),
            "wo": _w((lambda *s: torch.randn(*s, dtype=torch.bfloat16) * 0.05)(D, D)),
        }
    _A_in = _A_setup()

    def _B_setup():
        B, S, Dm, Df = 1, 2048, 512, 1376
        return {
            "x": _w((lambda *s: torch.randn(*s, dtype=torch.bfloat16) * 0.05)(B, S, Dm)),
            "w_gate": _w((lambda *s: torch.randn(*s, dtype=torch.bfloat16) * 0.8)(Dm, Df) * (Dm ** -0.5)),
            "w_up": _w((lambda *s: torch.randn(*s, dtype=torch.bfloat16) * 0.8)(Dm, Df) * (Dm ** -0.5)),
            "w_down": _w((lambda *s: torch.randn(*s, dtype=torch.bfloat16) * 0.8)(Df, Dm) * (Df ** -0.5)),
        }
    _B_in = _B_setup()

    return {
        "A_x": _A_in["x"],
        "A_wqkv": _A_in["wqkv"],
        "A_wo": _A_in["wo"],
        "B_x": _B_in["x"],
        "B_w_gate": _B_in["w_gate"],
        "B_w_up": _B_in["w_up"],
        "B_w_down": _B_in["w_down"],
    }

def reference(A_x, A_wqkv, A_wo, B_x, B_w_gate, B_w_up, B_w_down):
    def _A_ref(x, wqkv, wo):
        B, S, D = x.shape
        H = 32; Dh = D // H
        W, dil = 8, 2
        qkv = (lambda x, *d: x.permute(*d))((x @ wqkv).reshape(B, S, 3, H, Dh), 2, 0, 3, 1, 4)
        q, k, v = qkv[0], qkv[1], qkv[2]
        scale = Dh ** -0.5
        pos = torch.arange(S)
        off = torch.arange(W) * dil
        idx = pos[:, None] - off[None, :]
        mask = idx >= 0
        idx_c = torch.where(mask, idx, 0).reshape(-1)
        kg = k[:, :, idx_c, :].reshape(B, H, S, W, Dh)
        vg = v[:, :, idx_c, :].reshape(B, H, S, W, Dh)
        logits = torch.einsum("bhsd,bhswd->bhsw", q, kg) * scale
        logits = torch.where(mask[None, None, :, :], logits, -1e9)
        attn = _softmax(logits, axis=-1)
        o = torch.einsum("bhsw,bhswd->bhsd", attn, vg)
        return (lambda x, *d: x.permute(*d))(o, 0, 2, 1, 3).reshape(B, S, D) @ wo

    def _B_ref(x, w_gate, w_up, w_down):
        g = torch.einsum("bsd,df->bsf", x, w_gate)
        u = torch.einsum("bsd,df->bsf", x, w_up)
        h = _gelu(g) * u
        return torch.einsum("bsf,fd->bsd", h, w_down)

    x = A_x
    h = x + _A_ref(_layernorm(x, axis=-1), A_wqkv, A_wo)
    h2 = h + _B_ref(_layernorm(h, axis=-1), B_w_gate, B_w_up, B_w_down)
    return h2
```

```cpp
#include <hip/hip_runtime.h>
#include <hip/hip_bf16.h>
typedef unsigned short u16;
__device__ __forceinline__ float bf2f(u16 b){ return __uint_as_float(((unsigned)b)<<16); }
__device__ __forceinline__ u16 f2bf(float f){ unsigned u=__float_as_uint(f); u += 0x7FFFu + ((u>>16)&1u); return (u16)(u>>16); }

__global__ void ln_k(const u16* x, u16* y){
  int row=blockIdx.x; int t=threadIdx.x;
  float v[8]; float s=0;
  for(int j=0;j<8;j++){ v[j]=bf2f(x[row*512+t*8+j]); s+=v[j]; }
  for(int o=32;o>0;o>>=1) s+=__shfl_xor(s,o);
  float m=s/512.f; float q=0;
  for(int j=0;j<8;j++){ float d=v[j]-m; q+=d*d; }
  for(int o=32;o>0;o>>=1) q+=__shfl_xor(q,o);
  float r=rsqrtf(q/512.f+1e-5f);
  for(int j=0;j<8;j++) y[row*512+t*8+j]=f2bf((v[j]-m)*r);
}
__global__ void gemm_k(const u16* A,const u16* B,const u16* R,u16* C,int M,int N,int K){
  int n=blockIdx.x*64+threadIdx.x; int m=blockIdx.y*4+threadIdx.y;
  if(n>=N) return;
  float acc=0;
  for(int k=0;k<K;k++) acc+=bf2f(A[m*K+k])*bf2f(B[k*N+n]);
  if(R) acc=bf2f(f2bf(acc))+bf2f(R[m*N+n]);
  C[m*N+n]=f2bf(acc);
}
__global__ void attn_k(const u16* qkv,u16* o){
  int s=blockIdx.x; int h=threadIdx.x;
  float q[16]; for(int d=0;d<16;d++) q[d]=bf2f(qkv[s*1536+h*16+d]);
  float l[8]; float mx=-1e30f;
  for(int w=0;w<8;w++){ int p=s-2*w; float a=-1e9f; if(p>=0){ a=0; for(int d=0;d<16;d++) a+=q[d]*bf2f(qkv[p*1536+512+h*16+d]); a*=0.25f;} l[w]=a; mx=fmaxf(mx,a);}
  float sum=0; for(int w=0;w<8;w++){ l[w]=__expf(l[w]-mx); sum+=l[w]; }
  for(int d=0;d<16;d++){ float a=0; for(int w=0;w<8;w++){ int p=s-2*w; if(p>=0) a+=l[w]*bf2f(qkv[p*1536+1024+h*16+d]); } o[s*512+h*16+d]=f2bf(a/sum);}
}
__global__ void geglu_k(const u16* g,const u16* u,u16* a,int n){
  int i=blockIdx.x*256+threadIdx.x; if(i>=n) return;
  float x=bf2f(g[i]); float t=tanhf(0.7978845608f*(x+0.044715f*x*x*x)); float ge=0.5f*x*(1.f+t);
  a[i]=f2bf(bf2f(f2bf(ge))*bf2f(u[i]));
}
extern "C" void kernel_launch(void* const* d_in, const int* in_sizes, int n_in, void* d_out, int out_size, void* d_ws, size_t ws_size, hipStream_t stream){
  const u16* x=(const u16*)d_in[0]; const u16* wqkv=(const u16*)d_in[1]; const u16* wo=(const u16*)d_in[2];
  const u16* wg=(const u16*)d_in[4]; const u16* wu=(const u16*)d_in[5]; const u16* wd=(const u16*)d_in[6];
  u16* ws=(u16*)d_ws; u16* xn=ws; u16* qkv=xn+2048*512; u16* o=qkv+2048*1536; u16* h=o+2048*512; u16* hn=h+2048*512; u16* g=hn+2048*512; u16* u=g+2048*1376; u16* a=u+2048*1376;
  ln_k<<<2048,64,0,stream>>>(x,xn);
  gemm_k<<<dim3(1536/64,512),dim3(64,4),0,stream>>>(xn,wqkv,nullptr,qkv,2048,1536,512);
  attn_k<<<2048,32,0,stream>>>(qkv,o);
  gemm_k<<<dim3(8,512),dim3(64,4),0,stream>>>(o,wo,x,h,2048,512,512);
  ln_k<<<2048,64,0,stream>>>(h,hn);
  gemm_k<<<dim3(22,512),dim3(64,4),0,stream>>>(hn,wg,nullptr,g,2048,1376,512);
  gemm_k<<<dim3(22,512),dim3(64,4),0,stream>>>(hn,wu,nullptr,u,2048,1376,512);
  geglu_k<<<(2048*1376+255)/256,256,0,stream>>>(g,u,a,2048*1376);
  gemm_k<<<dim3(8,512),dim3(64,4),0,stream>>>(a,wd,h,(u16*)d_out,2048,512,1376);
}
```

```cpp
#include <hip/hip_runtime.h>
#include <hip/hip_bf16.h>

typedef unsigned v4u __attribute__((ext_vector_type(4)));
typedef unsigned v2u __attribute__((ext_vector_type(2)));
typedef float f32x4 __attribute__((ext_vector_type(4)));
typedef float f32x2 __attribute__((ext_vector_type(2)));
typedef short v4s __attribute__((ext_vector_type(4)));
typedef __bf16 v8bf __attribute__((ext_vector_type(8)));
typedef __bf16 v2bf __attribute__((ext_vector_type(2)));
#define LAS __attribute__((address_space(3)))
#define DEV __device__ __forceinline__
typedef __amdgpu_buffer_rsrc_t rsrc_t;

constexpr int SEQ = 2048, DM = 512, DF = 1376, NU = 172;
constexpr int OFF_W2T = 0;
constexpr int OFF_W3T = OFF_W2T + 8 * 16 * 4 * 1024;
constexpr int OFF_W4T = OFF_W3T + NU * 16 * 1024;
constexpr int OFF_O   = OFF_W4T + 8 * 43 * 4 * 1024;
constexpr int OFF_H   = OFF_O + 128 * 16 * 1024;
constexpr int OFF_ST  = OFF_H + 128 * 16 * 1024;
constexpr int OFF_ACT = OFF_ST + 128 * 1024;
constexpr int WS_USED = OFF_ACT + 128 * NU * 256;
constexpr int LDS_BYTES = 160 * 1024;

DEV rsrc_t mkrs(const void* p, int bytes) { return __builtin_amdgcn_make_buffer_rsrc((void*)p, 0, bytes, 0x00020000); }
template <int AUX> DEV v4u ld16(rsrc_t r, int voff, int soff) { return __builtin_amdgcn_raw_buffer_load_b128(r, voff, soff, AUX); }
template <int AUX> DEV void st16(v4u v, rsrc_t r, int voff, int soff) { __builtin_amdgcn_raw_buffer_store_b128(v, r, voff, soff, AUX); }
DEV f32x4 mf32(v4u a, v4u b, f32x4 c) { return __builtin_amdgcn_mfma_f32_16x16x32_bf16(__builtin_bit_cast(v8bf, a), __builtin_bit_cast(v8bf, b), c, 0, 0, 0); }
DEV f32x4 mf16(v2u a, v2u b, f32x4 c) { return __builtin_amdgcn_mfma_f32_16x16x16bf16_1k(__builtin_bit_cast(v4s, a), __builtin_bit_cast(v4s, b), c, 0, 0, 0); }
DEV unsigned pk2(float lo, float hi) { v2bf p; p[0] = (__bf16)lo; p[1] = (__bf16)hi; return __builtin_bit_cast(unsigned, p); }
DEV float bflo(unsigned u) { return __uint_as_float(u << 16); }
DEV float bfhi(unsigned u) { return __uint_as_float(u & 0xffff0000u); }
DEV float dot2(unsigned a, unsigned b, float c) { return __builtin_amdgcn_fdot2_f32_bf16(__builtin_bit_cast(v2bf, a), __builtin_bit_cast(v2bf, b), c, false); }
DEV v2u trrd(LAS char* p) { return __builtin_bit_cast(v2u, __builtin_amdgcn_ds_read_tr16_b64_v4i16((LAS v4s*)p)); }
DEV v4u trrd2(LAS char* p, int second) { v2u a = trrd(p), b = trrd(p + second); return v4u{a.x, a.y, b.x, b.y}; }
DEV v4u ldsr(LAS char* p) { return *(LAS v4u*)p; }
DEV void ldsw(LAS char* p, v4u v) { *(LAS v4u*)p = v; }
DEV void bar() { asm volatile("s_waitcnt lgkmcnt(0)" ::: "memory"); __builtin_amdgcn_s_barrier(); asm volatile("" ::: "memory"); }
template <int CTRL> DEV float dppf(float v) { return __builtin_bit_cast(float, __builtin_amdgcn_update_dpp(0, __builtin_bit_cast(int, v), CTRL, 0xf, 0xf, true)); }
DEV float x16add(float v) { auto r = __builtin_amdgcn_permlane16_swap(__float_as_uint(v), __float_as_uint(v), false, false); return __uint_as_float(r[0]) + __uint_as_float(r[1]); }
DEV float x32add(float v) { auto r = __builtin_amdgcn_permlane32_swap(__float_as_uint(v), __float_as_uint(v), false, false); return __uint_as_float(r[0]) + __uint_as_float(r[1]); }
DEV float x16max(float v) { auto r = __builtin_amdgcn_permlane16_swap(__float_as_uint(v), __float_as_uint(v), false, false); return fmaxf(__uint_as_float(r[0]), __uint_as_float(r[1])); }
DEV float x32max(float v) { auto r = __builtin_amdgcn_permlane32_swap(__float_as_uint(v), __float_as_uint(v), false, false); return fmaxf(__uint_as_float(r[0]), __uint_as_float(r[1])); }
constexpr unsigned ONES = 0x3f803f80u;

struct Params { const void *x, *wqkv, *wo, *wg, *wu, *wd; void *out, *ws; };

constexpr int L1_X = 0, L1_W = 80 * 1024, L1_PRE = 128 * 1024, L1_ST = 152 * 1024;

__global__ __launch_bounds__(256, 1) void k_p1(Params p) {
  extern __shared__ __attribute__((aligned(16))) char smem[];
  LAS char* lds = (LAS char*)smem;
  const int b = blockIdx.x, lane = threadIdx.x & 63, wave = __builtin_amdgcn_readfirstlane(threadIdx.x >> 6);
  const int R = (b & 7) * 4 + (b >> 6), j = (b >> 3) & 7;
  const rsrc_t rx = mkrs(p.x, SEQ * DM * 2), rw1 = mkrs(p.wqkv, DM * 3 * DM * 2), rws = mkrs(p.ws, WS_USED);
  const int g = lane >> 4, t = lane & 15;

  int kind; int tid2;
  if (wave == 0) { kind = 1; tid2 = b; }
  else if (wave == 1) { if (b < 96) { kind = 1; tid2 = 256 + b; } else { kind = 2; tid2 = b - 96; } }
  else if (wave == 2) { kind = 2; tid2 = 160 + b; }
  else { kind = b < 56 ? 2 : 0; tid2 = 416 + b; }
  v4u pre[8];
  if (kind == 1) {
    const int cb = tid2 >> 4, s = tid2 & 15;
    const rsrc_t rg = mkrs(p.wg, DM * DF * 2), ru = mkrs(p.wu, DM * DF * 2);
    const int vo = (lane >> 3) * (DF * 2) + (lane & 7) * 16, so = s * 32 * (DF * 2) + cb * 128;
#pragma unroll
    for (int q = 0; q < 4; q++) { pre[q] = ld16<0>(rg, vo, so + q * 8 * (DF * 2)); pre[4 + q] = ld16<0>(ru, vo, so + q * 8 * (DF * 2)); }
  } else if (kind == 2) {
    const bool is2 = tid2 < 128; const int tt = is2 ? tid2 : tid2 - 128;
    const rsrc_t rm = is2 ? mkrs(p.wo, DM * DM * 2) : mkrs(p.wd, DF * DM * 2);
    const int jj = tt & 7, s = tt >> 3;
    const int vo = (lane >> 3) * 1024 + (lane & 7) * 16, so = s * 32 * 1024 + jj * 128;
#pragma unroll
    for (int q = 0; q < 4; q++) pre[q] = ld16<0>(rm, vo, so + q * 8192);
  }
  int xvo[5], xla[5];
#pragma unroll
  for (int i = 0; i < 5; i++) {
    const int r80 = 20 * wave + 4 * i + g;
    int row = 64 * R - 16 + r80; row = row < 0 ? 0 : row;
    xvo[i] = row * 1024 + t * 16;
    const int sl = t >> 2, gg = t & 3;
    xla[i] = L1_X + (sl * 5 + (r80 >> 4)) * 1024 + (gg * 16 + ((r80 & 15) ^ (gg | ((sl & 1) << 2)))) * 16;
  }
  const int wvo = (lane >> 3) * 3072 + (lane & 7) * 16;
  v4u xr[4][5], wr[8][6];
  auto ldx = [&](int q) {
#pragma unroll
    for (int i = 0; i < 5; i++) xr[q][i] = ld16<0>(rx, xvo[i], q * 256);
  };
  auto ldw = [&](int c) {
#pragma unroll
    for (int i = 0; i < 6; i++) { const int idx = wave * 6 + i; wr[c][i] = ld16<0>(rw1, wvo, (64 * c + 8 * (idx & 7)) * 3072 + (idx >> 3) * 1024 + j * 128); }
  };
  ldx(0); ldw(0); ldw(1); ldx(1); ldw(2); ldw(3); ldx(2); ldw(4); ldw(5);
  __builtin_amdgcn_sched_barrier(0);

  {
    LAS char* sc = lds + L1_PRE + (wave < 2 ? wave * 8192 : 8192 + wave * 4096);
    const int q4 = (lane >> 2) & 3, p4 = lane & 3;
    if (kind == 1) {
      const int cb = tid2 >> 4, s = tid2 & 15;
#pragma unroll
      for (int q = 0; q < 4; q++) {
        LAS char* d = sc + (8 * q + (lane >> 3)) * 256 + (lane & 7) * 32;
        ldsw(d, v4u{pre[q].x, pre[4 + q].x, pre[q].y, pre[4 + q].y});
        ldsw(d + 16, v4u{pre[q].z, pre[4 + q].z, pre[q].w, pre[4 + q].w});
      }
      LAS char* ra = sc + (8 * g + q4) * 256 + p4 * 8;
      const int nun = cb == 21 ? 4 : 8;
#pragma unroll
      for (int c8 = 0; c8 < 8; c8++) {
        v4u f = trrd2(ra + c8 * 32, 1024);
        if (c8 < nun) st16<0>(f, rws, lane * 16, OFF_W3T + ((cb * 8 + c8) * 16 + s) * 1024);
      }
    } else if (kind == 2) {
      const bool is2 = tid2 < 128; const int tt = is2 ? tid2 : tid2 - 128;
      const int jj = tt & 7, s = tt >> 3;
#pragma unroll
      for (int q = 0; q < 4; q++) ldsw(sc + q * 1024 + lane * 16, pre[q]);
      LAS char* ra = sc + (8 * g + q4) * 128 + p4 * 16;
      const int dst = is2 ? OFF_W2T + (jj * 16 + s) * 4096 : OFF_W4T + (jj * 43 + s) * 4096;
#pragma unroll
      for (int n = 0; n < 4; n++) { v4u f = trrd2(ra + (n >> 1) * 64 + (n & 1) * 8, 512); st16<0>(f, rws, lane * 16, dst + n * 1024); }
    }
  }
  __builtin_amdgcn_sched_barrier(0);

  float ssum[5] = {0, 0, 0, 0, 0}, ssq[5] = {0, 0, 0, 0, 0};
  f32x4 aq[5], ak[5], av[5], cq = {0, 0, 0, 0}, ck = cq, cv = cq;
#pragma unroll
  for (int i = 0; i < 5; i++) { aq[i] = cq; ak[i] = cq; av[i] = cq; }
  const v4u ones = {ONES, ONES, ONES, ONES};
  const int rr = lane >> 3;
  const int wla0 = L1_W + rr * 128 + (((lane & 7) ^ (((rr >> 1) & 1) << 1)) << 4);
  const int wla1 = L1_W + rr * 128 + (((lane & 7) ^ ((((rr >> 1) & 1) | 2) << 1)) << 4);
  const int q4 = (lane >> 2) & 3, p4 = lane & 3;
  const int wra = L1_W + (8 * g + q4) * 128 + (((2 * wave + (p4 >> 1)) ^ ((((q4 >> 1) & 1) | ((g & 1) << 1)) << 1)) << 4) + (p4 & 1) * 8;
  const int xra0 = L1_X + (g * 16 + (t ^ g)) * 16, xra1 = L1_X + (g * 16 + (t ^ (g | 4))) * 16;
  auto stage = [&](int c) {
    if ((c & 1) == 0) {
      const int q = c >> 1;
#pragma unroll
      for (int i = 0; i < 5; i++) {
        const v4u v = xr[q][i];
        ldsw(lds + xla[i] + q * 20480, v);
        ssum[i] = dot2(v.x, ONES, ssum[i]); ssq[i] = dot2(v.x, v.x, ssq[i]);
        ssum[i] = dot2(v.y, ONES, ssum[i]); ssq[i] = dot2(v.y, v.y, ssq[i]);
        ssum[i] = dot2(v.z, ONES, ssum[i]); ssq[i] = dot2(v.z, v.z, ssq[i]);
        ssum[i] = dot2(v.w, ONES, ssum[i]); ssq[i] = dot2(v.w, v.w, ssq[i]);
      }
    }
#pragma unroll
    for (int i = 0; i < 6; i++) {
      const int idx = wave * 6 + i;
      ldsw(lds + ((i & 1) ? wla1 : wla0) + (c & 1) * 24576 + idx * 1024, wr[c][i]);
    }
  };
#pragma unroll
  for (int c = 0; c < 8; c++) {
    stage(c);
    if (c == 0) { ldx(3); ldw(6); }
    if (c == 1) { ldw(7); }
    if (c == 6) {
#pragma unroll
      for (int i = 0; i < 5; i++) {
        float a = ssum[i], q = ssq[i];
        a += dppf<0x128>(a); q += dppf<0x128>(q); a += dppf<0x124>(a); q += dppf<0x124>(q);
        a += dppf<0x122>(a); q += dppf<0x122>(q); a += dppf<0x121>(a); q += dppf<0x121>(q);
        const float mean = a * (1.f / 512.f);
        const float var = fmaxf(q * (1.f / 512.f) - mean * mean, 0.f);
        const float rstd = __builtin_amdgcn_rsqf(var + 1e-5f);
        if (t == 0) { const int r80 = 20 * wave + 4 * i + g; *(LAS float*)(lds + L1_ST + r80 * 4) = mean; *(LAS float*)(lds + L1_ST + 320 + r80 * 4) = rstd; }
      }
    }
    bar();
#pragma unroll
    for (int ks = 0; ks < 2; ks++) {
      const int s = 2 * c + ks;
      LAS char* wb = lds + wra + (c & 1) * 24576 + ks * 4096;
      const v4u fq = trrd2(wb, 512), fk = trrd2(wb + 8192, 512), fv = trrd2(wb + 16384, 512);
      LAS char* xb = lds + (ks ? xra1 : xra0) + s * 5120;
#pragma unroll
      for (int i = 0; i < 5; i++) {
        const v4u xf = ldsr(xb + i * 1024);
        if (i > 0) aq[i] = mf32(fq, xf, aq[i]);
        ak[i] = mf32(fk, xf, ak[i]);
        av[i] = mf32(xf, fv, av[i]);
      }
      cq = mf32(fq, ones, cq); ck = mf32(fk, ones, ck); cv = mf32(ones, fv, cv);
    }
    __builtin_amdgcn_sched_barrier(0);
  }

  v2u qb[5], kb[5], vb[5];
#pragma unroll
  for (int i = 0; i < 5; i++) {
    const float mt = *(LAS float*)(lds + L1_ST + (16 * i + t) * 4), rt = *(LAS float*)(lds + L1_ST + 320 + (16 * i + t) * 4);
    const f32x4 mv = *(LAS f32x4*)(lds + L1_ST + (16 * i + 4 * g) * 4), rv = *(LAS f32x4*)(lds + L1_ST + 320 + (16 * i + 4 * g) * 4);
    float q[4], k[4], v[4];
#pragma unroll
    for (int r = 0; r < 4; r++) { q[r] = (aq[i][r] - mt * cq[r]) * rt; k[r] = (ak[i][r] - mt * ck[r]) * rt; v[r] = (av[i][r] - mv[r] * cv[r]) * rv[r]; }
    qb[i] = v2u{pk2(q[0], q[1]), pk2(q[2], q[3])}; kb[i] = v2u{pk2(k[0], k[1]), pk2(k[2], k[3])}; vb[i] = v2u{pk2(v[0], v[1]), pk2(v[2], v[3])};
  }
  const int pe = t & 1;
  const int d1a = t - 4 * g - pe;
  const float NINF = -__builtin_inff();
  const float b1a = d1a >= 0 ? 0.f : NINF, b1b = d1a - 2 >= 0 ? 0.f : NINF;
  const float b0a = d1a + 16 <= 14 ? 0.f : NINF, b0b = d1a + 14 <= 14 ? 0.f : NINF;
  const float SC = 0.25f * 1.4426950408889634f;
  v2u ob[5];
#pragma unroll
  for (int i = 1; i < 5; i++) {
    const f32x4 z = {0, 0, 0, 0};
    const f32x4 s0 = mf16(kb[i - 1], qb[i], z), s1 = mf16(kb[i], qb[i], z);
    const bool first = (i == 1) && (R == 0);
    float e0a = fmaf(pe ? s0[1] : s0[0], SC, first ? NINF : b0a), e0b = fmaf(pe ? s0[3] : s0[2], SC, first ? NINF : b0b);
    float e1a = fmaf(pe ? s1[1] : s1[0], SC, b1a), e1b = fmaf(pe ? s1[3] : s1[2], SC, b1b);
    float m = fmaxf(fmaxf(e0a, e0b), fmaxf(e1a, e1b));
    m = x32max(x16max(m));
    e0a = __builtin_amdgcn_exp2f(e0a - m); e0b = __builtin_amdgcn_exp2f(e0b - m);
    e1a = __builtin_amdgcn_exp2f(e1a - m); e1b = __builtin_amdgcn_exp2f(e1b - m);
    float sum = (e0a + e0b) + (e1a + e1b);
    sum = x32add(x16add(sum));
    const int sh = pe * 16;
    const v4u pp = {pk2(e0a, 0.f) << sh, pk2(e0b, 0.f) << sh, pk2(e1a, 0.f) << sh, pk2(e1b, 0.f) << sh};
    const v4u vv = {vb[i - 1].x, vb[i - 1].y, vb[i].x, vb[i].y};
    const f32x4 o = mf32(vv, pp, z);
    const float inv = __builtin_amdgcn_rcpf(sum);
    ob[i] = v2u{pk2(o[0] * inv, o[1] * inv), pk2(o[2] * inv, o[3] * inv)};
  }
  const int hg = 4 * j + wave;
#pragma unroll
  for (int pr = 0; pr < 2; pr++) {
    const int a = 1 + 2 * pr;
    auto rx0 = __builtin_amdgcn_permlane16_swap(ob[a].x, ob[a + 1].x, false, false);
    auto rx1 = __builtin_amdgcn_permlane16_swap(ob[a].y, ob[a + 1].y, false, false);
    const v4u v = {rx0[0], rx1[0], rx0[1], rx1[1]};
    const int mt = 4 * R + 2 * pr + (g & 1), g4 = 2 * (hg & 1) + (g >> 1);
    st16<0>(v, rws, mt * 16384 + (g4 * 16 + t) * 16, OFF_O + (hg >> 1) * 1024);
  }
}

DEV void reduce4(LAS char* lds, int wave, int lane, f32x4 (&acc)[4][4], f32x4 (&res)[4]) {
#pragma unroll
  for (int m = 0; m < 4; m++)
#pragma unroll
    for (int n = 0; n < 4; n++) *(LAS f32x4*)(lds + ((m * 4 + wave) * 4 + n) * 1024 + lane * 16) = acc[m][n];
  bar();
#pragma unroll
  for (int n = 0; n < 4; n++) {
    f32x4 s = *(LAS f32x4*)(lds + ((wave * 4 + 0) * 4 + n) * 1024 + lane * 16);
#pragma unroll
    for (int w = 1; w < 4; w++) s += *(LAS f32x4*)(lds + ((wave * 4 + w) * 4 + n) * 1024 + lane * 16);
    res[n] = s;
  }
}

__global__ __launch_bounds__(256, 1) void k_p2(Params p) {
  extern __shared__ __attribute__((aligned(16))) char smem[];
  LAS char* lds = (LAS char*)smem;
  const int b = blockIdx.x, lane = threadIdx.x & 63, wave = __builtin_amdgcn_readfirstlane(threadIdx.x >> 6);
  const int R = (b & 7) * 4 + (b >> 6), j = (b >> 3) & 7;
  const int g = lane >> 4, t = lane & 15;
  const rsrc_t rx = mkrs(p.x, SEQ * DM * 2), rws = mkrs(p.ws, WS_USED);
  v4u wf[4][4], of[4][4], xres[2];
#pragma unroll
  for (int s = 0; s < 4; s++) {
#pragma unroll
    for (int n = 0; n < 4; n++) wf[s][n] = ld16<0>(rws, lane * 16, OFF_W2T + ((j * 16 + 4 * wave + s) * 4 + n) * 1024);
#pragma unroll
    for (int m = 0; m < 4; m++) of[s][m] = ld16<0>(rws, lane * 16, OFF_O + ((4 * R + m) * 16 + 4 * wave + s) * 1024);
  }
#pragma unroll
  for (int mm = 0; mm < 2; mm++) xres[mm] = ld16<0>(rx, t * 1024 + g * 16, (64 * R + 16 * wave) * 1024 + j * 128 + mm * 64);
  __builtin_amdgcn_sched_barrier(0);
  f32x4 acc[4][4] = {};
#pragma unroll
  for (int s = 0; s < 4; s++)
#pragma unroll
    for (int m = 0; m < 4; m++)
#pragma unroll
      for (int n = 0; n < 4; n++) acc[m][n] = mf32(wf[s][n], of[s][m], acc[m][n]);
  f32x4 res[4];
  reduce4(lds, wave, lane, acc, res);
  float sm = 0.f, sq = 0.f;
#pragma unroll
  for (int mm = 0; mm < 2; mm++) {
    const v4u x = xres[mm]; const f32x4 a = res[2 * mm], c = res[2 * mm + 1];
    const v4u h = {pk2(a[0] + bflo(x.x), a[1] + bfhi(x.x)), pk2(a[2] + bflo(x.y), a[3] + bfhi(x.y)),
                   pk2(c[0] + bflo(x.z), c[1] + bfhi(x.z)), pk2(c[2] + bflo(x.w), c[3] + bfhi(x.w))};
    st16<0>(h, rws, lane * 16, OFF_H + ((4 * R + wave) * 16 + 2 * j + mm) * 1024);
    sm = dot2(h.x, ONES, sm); sq = dot2(h.x, h.x, sq); sm = dot2(h.y, ONES, sm); sq = dot2(h.y, h.y, sq);
    sm = dot2(h.z, ONES, sm); sq = dot2(h.z, h.z, sq); sm = dot2(h.w, ONES, sm); sq = dot2(h.w, h.w, sq);
  }
  sm = x32add(x16add(sm)); sq = x32add(x16add(sq));
  if (g == 0) __builtin_amdgcn_raw_buffer_store_b64(v2u{__float_as_uint(sm), __float_as_uint(sq)}, rws, t * 8, OFF_ST + (4 * R + wave) * 1024 + j * 128, 0);
}

constexpr int L3_A = 0, L3_C = 128 * 1024;
__global__ __launch_bounds__(256, 1) void k_p3(Params p) {
  extern __shared__ __attribute__((aligned(16))) char smem[];
  LAS char* lds = (LAS char*)smem;
  const int b = blockIdx.x, lane = threadIdx.x & 63, wave = __builtin_amdgcn_readfirstlane(threadIdx.x >> 6);
  const int x = b & 7, i = b >> 3;
  const int P = 8 * (x >> 2) + (i >> 2), j16 = 4 * (x & 3) + (i & 3);
  const int g = lane >> 4, t = lane & 15;
  const rsrc_t rws = mkrs(p.ws, WS_USED);
  const int ub = 11 * j16 - (j16 > 12 ? j16 - 12 : 0);
  const bool small = j16 >= 12;
  const int u0 = ub + ((small && wave == 3) ? 8 : 3 * wave);
  const int nf = (wave == 3 || (small && wave == 2)) ? 2 : 3;
  v4u sv[2], hs[8][4], B[3][16];
#pragma unroll
  for (int k = 0; k < 2; k++) sv[k] = ld16<0>(rws, lane * 16, OFF_ST + (8 * P + 2 * wave + k) * 1024);
#pragma unroll
  for (int k = 0; k < 4; k++) hs[0][k] = ld16<0>(rws, lane * 16, OFF_H + ((8 * P + 0) * 16 + 4 * wave + k) * 1024);
#pragma unroll
  for (int s = 0; s < 16; s++) {
    B[0][s] = ld16<0>(rws, lane * 16, OFF_W3T + ((u0 + 0) * 16 + s) * 1024);
    B[1][s] = ld16<0>(rws, lane * 16, OFF_W3T + ((u0 + 1) * 16 + s) * 1024);
    B[2][s] = ld16<0>(rws, lane * 16, OFF_W3T + ((u0 + (nf == 3 ? 2 : 1)) * 16 + s) * 1024);
  }
#pragma unroll
  for (int m = 1; m < 4; m++)
#pragma unroll
    for (int k = 0; k < 4; k++) hs[m][k] = ld16<0>(rws, lane * 16, OFF_H + ((8 * P + m) * 16 + 4 * wave + k) * 1024);
  __builtin_amdgcn_sched_barrier(0);
#pragma unroll
  for (int k = 0; k < 2; k++) {
    float v[4] = {__uint_as_float(sv[k].x), __uint_as_float(sv[k].y), __uint_as_float(sv[k].z), __uint_as_float(sv[k].w)};
#pragma unroll
    for (int e = 0; e < 4; e++) { v[e] += dppf<0x128>(v[e]); v[e] = x32add(x16add(v[e])); }
#pragma unroll
    for (int e = 0; e < 2; e++) {
      const float mean = v[2 * e] * (1.f / 512.f);
      const float irs2 = fmaxf(v[2 * e + 1] * (1.f / 512.f) - mean * mean, 0.f) + 1e-5f;
      const float rstd = __builtin_amdgcn_rsqf(irs2);
      const float k1 = -2.3022082f * rstd, k2 = -0.10294324f * rstd * rstd * rstd;
      if (lane < 8) *(LAS f32x4*)(lds + L3_C + ((2 * wave + k) * 16 + 2 * lane + e) * 16) = f32x4{mean, k1, k2, irs2};
    }
  }
  const v4u ones = {ONES, ONES, ONES, ONES};
  f32x4 cs[3] = {};
#pragma unroll
  for (int s = 0; s < 16; s++)
#pragma unroll
    for (int f = 0; f < 3; f++) cs[f] = mf32(B[f][s], ones, cs[f]);
#pragma unroll
  for (int mt = 0; mt < 8; mt++) {
#pragma unroll
    for (int k = 0; k < 4; k++) ldsw(lds + L3_A + (mt * 16 + 4 * wave + k) * 1024 + lane * 16, hs[mt][k]);
    if (mt + 4 < 8) {
#pragma unroll
      for (int k = 0; k < 4; k++) hs[mt + 4][k] = ld16<0>(rws, lane * 16, OFF_H + ((8 * P + mt + 4) * 16 + 4 * wave + k) * 1024);
    }
    bar();
    f32x4 acc[3] = {};
#pragma unroll
    for (int s = 0; s < 16; s++) {
      const v4u a = ldsr(lds + L3_A + (mt * 16 + s) * 1024 + lane * 16);
#pragma unroll
      for (int f = 0; f < 3; f++) acc[f] = mf32(B[f][s], a, acc[f]);
    }
    const f32x4 cc = *(LAS f32x4*)(lds + L3_C + (mt * 16 + t) * 16);
    unsigned d[4];
#pragma unroll
    for (int f = 0; f < 3; f++) {
      float o[2];
#pragma unroll
      for (int e = 0; e < 2; e++) {
        const float tg = fmaf(-cc[0], cs[f][e], acc[f][e]), tu = fmaf(-cc[0], cs[f][2 + e], acc[f][2 + e]);
        const float a = tg * fmaf(tg * tg, cc[2], cc[1]);
        const float ex = __builtin_amdgcn_exp2f(a);
        o[e] = (tg * tu) * __builtin_amdgcn_rcpf(fmaf(ex, cc[3], cc[3]));
      }
      d[f] = pk2(o[0], o[1]);
    }
    d[3] = 0;
    auto r01 = __builtin_amdgcn_permlane16_swap(d[0], d[1], false, false);
    auto r23 = __builtin_amdgcn_permlane16_swap(d[2], d[3], false, false);
    auto r02 = __builtin_amdgcn_permlane32_swap(r01[0], r23[0], false, false);
    auto r13 = __builtin_amdgcn_permlane32_swap(r01[1], r23[1], false, false);
    const v4u v = {r02[0], r13[0], r02[1], r13[1]};
    if (g < nf) st16<0>(v, rws, g * 256 + t * 16, OFF_ACT + ((8 * P + mt) * NU + u0) * 256);
    __builtin_amdgcn_sched_barrier(0);
  }
}

__global__ __launch_bounds__(256, 1) void k_p4(Params p) {
  extern __shared__ __attribute__((aligned(16))) char smem[];
  LAS char* lds = (LAS char*)smem;
  const int b = blockIdx.x, lane = threadIdx.x & 63, wave = __builtin_amdgcn_readfirstlane(threadIdx.x >> 6);
  const int R = (b & 7) * 4 + (b >> 6), j = (b >> 3) & 7;
  const int g = lane >> 4, t = lane & 15;
  const rsrc_t ro = mkrs(p.out, SEQ * DM * 2), rws = mkrs(p.ws, WS_USED);
  const int s0 = 11 * wave; const bool full = wave < 3;
  v4u wf[11][4], af[11][4], hres[2];
#pragma unroll
  for (int mm = 0; mm < 2; mm++) hres[mm] = ld16<0>(rws, lane * 16, OFF_H + ((4 * R + wave) * 16 + 2 * j + mm) * 1024);
#pragma unroll
  for (int s = 0; s < 11; s++) {
    const int ss = (s < 10 || full) ? s0 + s : s0;
#pragma unroll
    for (int n = 0; n < 4; n++) wf[s][n] = ld16<0>(rws, lane * 16, OFF_W4T + ((j * 43 + ss) * 4 + n) * 1024);
#pragma unroll
    for (int m = 0; m < 4; m++) af[s][m] = ld16<0>(rws, lane * 16, OFF_ACT + ((4 * R + m) * NU + 4 * ss) * 256);
  }
  __builtin_amdgcn_sched_barrier(0);
  f32x4 acc[4][4] = {};
#pragma unroll
  for (int s = 0; s < 11; s++) {
    if (s < 10 || full) {
#pragma unroll
      for (int m = 0; m < 4; m++)
#pragma unroll
        for (int n = 0; n < 4; n++) acc[m][n] = mf32(wf[s][n], af[s][m], acc[m][n]);
    }
  }
  f32x4 res[4];
  reduce4(lds, wave, lane, acc, res);
#pragma unroll
  for (int mm = 0; mm < 2; mm++) {
    const v4u x = hres[mm]; const f32x4 a = res[2 * mm], c = res[2 * mm + 1];
    const v4u h = {pk2(a[0] + bflo(x.x), a[1] + bfhi(x.x)), pk2(a[2] + bflo(x.y), a[3] + bfhi(x.y)),
                   pk2(c[0] + bflo(x.z), c[1] + bfhi(x.z)), pk2(c[2] + bflo(x.w), c[3] + bfhi(x.w))};
    st16<0>(h, ro, t * 1024 + g * 16, (64 * R + 16 * wave) * 1024 + j * 128 + mm * 64);
  }
}

extern "C" void kernel_launch(void* const* d_in, const int* in_sizes, int n_in, void* d_out, int out_size, void* d_ws, size_t ws_size, hipStream_t stream) {
  Params p{d_in[0], d_in[1], d_in[2], d_in[4], d_in[5], d_in[6], d_out, d_ws};
  (void)hipFuncSetAttribute((const void*)k_p1, hipFuncAttributeMaxDynamicSharedMemorySize, LDS_BYTES);
  (void)hipFuncSetAttribute((const void*)k_p2, hipFuncAttributeMaxDynamicSharedMemorySize, LDS_BYTES);
  (void)hipFuncSetAttribute((const void*)k_p3, hipFuncAttributeMaxDynamicSharedMemorySize, LDS_BYTES);
  (void)hipFuncSetAttribute((const void*)k_p4, hipFuncAttributeMaxDynamicSharedMemorySize, LDS_BYTES);
  k_p1<<<256, 256, LDS_BYTES, stream>>>(p);
  k_p2<<<256, 256, LDS_BYTES, stream>>>(p);
  k_p3<<<256, 256, LDS_BYTES, stream>>>(p);
  k_p4<<<256, 256, LDS_BYTES, stream>>>(p);
}
```

```cpp
#include <hip/hip_runtime.h>
#include <hip/hip_bf16.h>
struct Params { const void *x, *wqkv, *wo, *wg, *wu, *wd; void *out, *ws; };
__global__ __launch_bounds__(512, 2) void fused_block(Params p) {
  unsigned tid = threadIdx.x;
  asm volatile(
    "s_load_dwordx16 s[8:23], %1, 0x0\n"
    "v_lshrrev_b32 v20, 6, v0\n"
    "v_and_b32 v0, 63, v0\n"
    "s_mov_b32 s48, %2\n"
    "v_readfirstlane_b32 s49, v20\n"
    "v_lshlrev_b32 v1, 4, v0\n"
    "v_lshrrev_b32 v2, 4, v0\n"
    "v_and_b32 v3, 15, v0\n"
    "s_and_b32 s53, s49, 3\n"
    "s_lshr_b32 s57, s49, 2\n"
    "s_and_b32 s58, s48, 7\n"
    "s_lshl_b32 s50, s58, 2\n"
    "s_lshr_b32 s60, s48, 6\n"
    "s_add_u32 s50, s50, s60\n"
    "s_bfe_u32 s51, s48, 0x30003\n"
    "v_lshrrev_b32 v24, 3, v0\n"
    "v_and_b32 v25, 7, v0\n"
    "v_lshlrev_b32 v25, 4, v25\n"
    "s_cmp_eq_u32 s57, 0\n"
    "s_nop 1\n"
    "s_cbranch_scc0 .Lp1_b\n"
    "s_mul_i32 s60, s53, 20\n"
    "s_lshl_b32 s61, s50, 6\n"
    "s_sub_u32 s61, s61, 16\n"
    "v_lshrrev_b32 v20, 5, v0\n"
    "v_add_u32 v20, s60, v20\n"
    "v_and_b32 v21, 31, v0\n"
    "v_and_b32 v22, 15, v20\n"
    "v_xor_b32 v22, v22, v21\n"
    "v_lshlrev_b32 v22, 4, v22\n"
    "v_add_u32 v23, s61, v20\n"
    "v_max_i32 v23, 0, v23\n"
    "v_lshl_add_u32 v160, v23, 10, v22\n"
    "v_add_u32 v20, 2, v20\n"
    "v_and_b32 v22, 15, v20\n"
    "v_xor_b32 v22, v22, v21\n"
    "v_lshlrev_b32 v22, 4, v22\n"
    "v_add_u32 v23, s61, v20\n"
    "v_max_i32 v23, 0, v23\n"
    "v_lshl_add_u32 v161, v23, 10, v22\n"
    "v_add_u32 v20, 2, v20\n"
    "v_and_b32 v22, 15, v20\n"
    "v_xor_b32 v22, v22, v21\n"
    "v_lshlrev_b32 v22, 4, v22\n"
    "v_add_u32 v23, s61, v20\n"
    "v_max_i32 v23, 0, v23\n"
    "v_lshl_add_u32 v162, v23, 10, v22\n"
    "v_add_u32 v20, 2, v20\n"
    "v_and_b32 v22, 15, v20\n"
    "v_xor_b32 v22, v22, v21\n"
    "v_lshlrev_b32 v22, 4, v22\n"
    "v_add_u32 v23, s61, v20\n"
    "v_max_i32 v23, 0, v23\n"
    "v_lshl_add_u32 v163, v23, 10, v22\n"
    "v_add_u32 v20, 2, v20\n"
    "v_and_b32 v22, 15, v20\n"
    "v_xor_b32 v22, v22, v21\n"
    "v_lshlrev_b32 v22, 4, v22\n"
    "v_add_u32 v23, s61, v20\n"
    "v_max_i32 v23, 0, v23\n"
    "v_lshl_add_u32 v164, v23, 10, v22\n"
    "v_add_u32 v20, 2, v20\n"
    "v_and_b32 v22, 15, v20\n"
    "v_xor_b32 v22, v22, v21\n"
    "v_lshlrev_b32 v22, 4, v22\n"
    "v_add_u32 v23, s61, v20\n"
    "v_max_i32 v23, 0, v23\n"
    "v_lshl_add_u32 v165, v23, 10, v22\n"
    "v_add_u32 v20, 2, v20\n"
    "v_and_b32 v22, 15, v20\n"
    "v_xor_b32 v22, v22, v21\n"
    "v_lshlrev_b32 v22, 4, v22\n"
    "v_add_u32 v23, s61, v20\n"
    "v_max_i32 v23, 0, v23\n"
    "v_lshl_add_u32 v166, v23, 10, v22\n"
    "v_add_u32 v20, 2, v20\n"
    "v_and_b32 v22, 15, v20\n"
    "v_xor_b32 v22, v22, v21\n"
    "v_lshlrev_b32 v22, 4, v22\n"
    "v_add_u32 v23, s61, v20\n"
    "v_max_i32 v23, 0, v23\n"
    "v_lshl_add_u32 v167, v23, 10, v22\n"
    "v_add_u32 v20, 2, v20\n"
    "v_and_b32 v22, 15, v20\n"
    "v_xor_b32 v22, v22, v21\n"
    "v_lshlrev_b32 v22, 4, v22\n"
    "v_add_u32 v23, s61, v20\n"
    "v_max_i32 v23, 0, v23\n"
    "v_lshl_add_u32 v168, v23, 10, v22\n"
    "v_add_u32 v20, 2, v20\n"
    "v_and_b32 v22, 15, v20\n"
    "v_xor_b32 v22, v22, v21\n"
    "v_lshlrev_b32 v22, 4, v22\n"
    "v_add_u32 v23, s61, v20\n"
    "v_max_i32 v23, 0, v23\n"
    "v_lshl_add_u32 v169, v23, 10, v22\n"
    "s_mul_i32 s33, s53, 0x2800\n"
    "s_waitcnt lgkmcnt(0)\n"
    "s_mov_b32 s24, s8\n"
    "s_and_b32 s25, s9, 0xffff\n"
    "s_mov_b32 s26, 0x200000\n"
    "s_mov_b32 s27, 0x20000\n"
    "s_add_u32 m0, s33, 0\n"
    "s_nop 0\n"
    "buffer_load_dwordx4 v160, s[24:27], 0 offen lds\n"
    "s_add_u32 m0, s33, 0x400\n"
    "s_nop 0\n"
    "buffer_load_dwordx4 v161, s[24:27], 0 offen lds\n"
    "s_add_u32 m0, s33, 0x800\n"
    "s_nop 0\n"
    "buffer_load_dwordx4 v162, s[24:27], 0 offen lds\n"
    "s_add_u32 m0, s33, 0xc00\n"
    "s_nop 0\n"
    "buffer_load_dwordx4 v163, s[24:27], 0 offen lds\n"
    "s_add_u32 m0, s33, 0x1000\n"
    "s_nop 0\n"
    "buffer_load_dwordx4 v164, s[24:27], 0 offen lds\n"
    "s_add_u32 m0, s33, 0x1400\n"
    "s_nop 0\n"
    "buffer_load_dwordx4 v165, s[24:27], 0 offen lds\n"
    "s_add_u32 m0, s33, 0x1800\n"
    "s_nop 0\n"
    "buffer_load_dwordx4 v166, s[24:27], 0 offen lds\n"
    "s_add_u32 m0, s33, 0x1c00\n"
    "s_nop 0\n"
    "buffer_load_dwordx4 v167, s[24:27], 0 offen lds\n"
    "s_add_u32 m0, s33, 0x2000\n"
    "s_nop 0\n"
    "buffer_load_dwordx4 v168, s[24:27], 0 offen lds\n"
    "s_add_u32 m0, s33, 0x2400\n"
    "s_nop 0\n"
    "buffer_load_dwordx4 v169, s[24:27], 0 offen lds\n"
    "v_and_b32 v20, 2, v24\n"
    "v_lshlrev_b32 v20, 4, v20\n"
    "v_xor_b32 v20, v20, v25\n"
    "v_mul_u32_u24 v21, 0xc00, v24\n"
    "v_add_u32 v170, v21, v20\n"
    "v_xor_b32 v171, 64, v170\n"
    "s_mul_i32 s62, s53, 6\n"
    "s_lshl_b32 s63, s51, 7\n"
    "s_add_u32 s60, s62, 0\n"
    "s_and_b32 s61, s60, 7\n"
    "s_mul_i32 s61, s61, 0x6000\n"
    "s_lshr_b32 s60, s60, 3\n"
    "s_lshl_b32 s60, s60, 10\n"
    "s_add_u32 s61, s61, s60\n"
    "s_add_u32 s66, s61, s63\n"
    "s_add_u32 s60, s62, 1\n"
    "s_and_b32 s61, s60, 7\n"
    "s_mul_i32 s61, s61, 0x6000\n"
    "s_lshr_b32 s60, s60, 3\n"
    "s_lshl_b32 s60, s60, 10\n"
    "s_add_u32 s61, s61, s60\n"
    "s_add_u32 s67, s61, s63\n"
    "s_add_u32 s60, s62, 2\n"
    "s_and_b32 s61, s60, 7\n"
    "s_mul_i32 s61, s61, 0x6000\n"
    "s_lshr_b32 s60, s60, 3\n"
    "s_lshl_b32 s60, s60, 10\n"
    "s_add_u32 s61, s61, s60\n"
    "s_add_u32 s68, s61, s63\n"
    "s_add_u32 s60, s62, 3\n"
    "s_and_b32 s61, s60, 7\n"
    "s_mul_i32 s61, s61, 0x6000\n"
    "s_lshr_b32 s60, s60, 3\n"
    "s_lshl_b32 s60, s60, 10\n"
    "s_add_u32 s61, s61, s60\n"
    "s_add_u32 s69, s61, s63\n"
    "s_add_u32 s60, s62, 4\n"
    "s_and_b32 s61, s60, 7\n"
    "s_mul_i32 s61, s61, 0x6000\n"
    "s_lshr_b32 s60, s60, 3\n"
    "s_lshl_b32 s60, s60, 10\n"
    "s_add_u32 s61, s61, s60\n"
    "s_add_u32 s70, s61, s63\n"
    "s_add_u32 s60, s62, 5\n"
    "s_and_b32 s61, s60, 7\n"
    "s_mul_i32 s61, s61, 0x6000\n"
    "s_lshr_b32 s60, s60, 3\n"
    "s_lshl_b32 s60, s60, 10\n"
    "s_add_u32 s61, s61, s60\n"
    "s_add_u32 s71, s61, s63\n"
    "s_mul_i32 s34, s53, 0x1800\n"
    "s_add_u32 s34, s34, 0x14000\n"
    "s_mov_b32 s28, s10\n"
    "s_and_b32 s29, s11, 0xffff\n"
    "s_mov_b32 s30, 0x180000\n"
    "s_mov_b32 s31, 0x20000\n"
    "s_add_u32 m0, s34, 0\n"
    "s_nop 0\n"
    "buffer_load_dwordx4 v170, s[28:31], s66 offen lds\n"
    "s_add_u32 s66, s66, 0x30000\n"
    "s_add_u32 m0, s34, 0x400\n"
    "s_nop 0\n"
    "buffer_load_dwordx4 v171, s[28:31], s67 offen lds\n"
    "s_add_u32 s67, s67, 0x30000\n"
    "s_add_u32 m0, s34, 0x800\n"
    "s_nop 0\n"
    "buffer_load_dwordx4 v170, s[28:31], s68 offen lds\n"
    "s_add_u32 s68, s68, 0x30000\n"
    "s_add_u32 m0, s34, 0xc00\n"
    "s_nop 0\n"
    "buffer_load_dwordx4 v171, s[28:31], s69 offen lds\n"
    "s_add_u32 s69, s69, 0x30000\n"
    "s_add_u32 m0, s34, 0x1000\n"
    "s_nop 0\n"
    "buffer_load_dwordx4 v170, s[28:31], s70 offen lds\n"
    "s_add_u32 s70, s70, 0x30000\n"
    "s_add_u32 m0, s34, 0x1400\n"
    "s_nop 0\n"
    "buffer_load_dwordx4 v171, s[28:31], s71 offen lds\n"
    "s_add_u32 s71, s71, 0x30000\n"
    "s_mov_b32 s36, s22\n"
    "s_and_b32 s37, s23, 0xffff\n"
    "s_mov_b32 s38, 0xe89000\n"
    "s_mov_b32 s39, 0x20000\n"
    "s_add_u32 s58, s22, 0xe88000\n"
    "s_addc_u32 s59, s23, 0\n"
    "s_load_dword s56, s[58:59], 0x0\n"
    "s_waitcnt lgkmcnt(0)\n"
    "s_mov_b32 s40, s20\n"
    "s_and_b32 s41, s21, 0xffff\n"
    "s_mov_b32 s42, 0x200000\n"
    "s_mov_b32 s43, 0x20000\n"
    "s_and_b32 s58, s48, 7\n"
    "s_lshr_b32 s59, s48, 3\n"
    "s_lshr_b32 s52, s58, 2\n"
    "s_lshl_b32 s52, s52, 3\n"
    "s_lshr_b32 s60, s59, 2\n"
    "s_add_u32 s52, s52, s60\n"
    "s_and_b32 s62, s58, 3\n"
    "s_lshl_b32 s62, s62, 2\n"
    "s_and_b32 s60, s59, 3\n"
    "s_add_u32 s62, s62, s60\n"
    "s_mul_i32 s54, s62, 11\n"
    "s_sub_i32 s60, s62, 12\n"
    "s_max_i32 s60, s60, 0\n"
    "s_sub_u32 s54, s54, s60\n"
    "s_mul_i32 s60, s53, 3\n"
    "s_add_u32 s54, s54, s60\n"
    "s_cmp_ge_u32 s62, 12\n"
    "s_cselect_b32 s61, 1, 0\n"
    "s_cmp_eq_u32 s53, 3\n"
    "s_cselect_b32 s63, s61, 0\n"
    "s_sub_u32 s54, s54, s63\n"
    "s_add_u32 s63, s53, s61\n"
    "s_cmp_ge_u32 s63, 3\n"
    "s_cselect_b32 s55, 2, 3\n"
    "v_mov_b32 v32, 0x3f803f80\n"
    "v_mov_b32 v33, 0x3f803f80\n"
    "v_mov_b32 v34, 0x3f803f80\n"
    "v_mov_b32 v35, 0x3f803f80\n"
    "s_mov_b32 s74, 0x10001\n"
    "s_mov_b32 s75, 0x10001\n"
    "s_lshl_b32 s60, s57, 14\n"
    "s_add_u32 s61, s53, 1\n"
    "s_mul_i32 s61, s61, s57\n"
    "s_lshl_b32 s61, s61, 13\n"
    "v_xor_b32 v20, v2, v3\n"
    "v_lshlrev_b32 v20, 4, v20\n"
    "v_lshl_add_u32 v20, v3, 9, v20\n"
    "v_xor_b32 v21, 0, v20\n"
    "v_add_u32 v12, s60, v21\n"
    "v_add_u32 v16, s61, v21\n"
    "v_xor_b32 v21, 64, v20\n"
    "v_add_u32 v13, s60, v21\n"
    "v_add_u32 v17, s61, v21\n"
    "v_xor_b32 v21, 0x80, v20\n"
    "v_add_u32 v14, s60, v21\n"
    "v_add_u32 v18, s61, v21\n"
    "v_xor_b32 v21, 0xc0, v20\n"
    "v_add_u32 v15, s60, v21\n"
    "v_add_u32 v19, s61, v21\n"
    "v_bfe_u32 v27, v0, 2, 2\n"
    "v_and_b32 v28, 3, v0\n"
    "v_lshrrev_b32 v20, 1, v28\n"
    "s_lshl_b32 s60, s53, 1\n"
    "v_add_u32 v20, s60, v20\n"
    "v_and_b32 v21, 2, v27\n"
    "v_xor_b32 v20, v20, v21\n"
    "v_and_b32 v21, 1, v2\n"
    "v_lshlrev_b32 v21, 2, v21\n"
    "v_xor_b32 v20, v20, v21\n"
    "v_lshlrev_b32 v20, 4, v20\n"
    "v_and_b32 v21, 1, v28\n"
    "v_lshl_add_u32 v20, v21, 3, v20\n"
    "v_lshl_add_u32 v29, v2, 3, v27\n"
    "v_lshl_add_u32 v10, v29, 7, v20\n"
    "v_add_u32 v10, 0x14000, v10\n"
    "v_add_u32 v11, 0xc000, v10\n"
    "s_add_u32 s56, s56, 1\n"
    "s_add_u32 m0, s34, 0x6000\n"
    "s_nop 0\n"
    "buffer_load_dwordx4 v170, s[28:31], s66 offen lds\n"
    "s_add_u32 s66, s66, 0x30000\n"
    "s_add_u32 m0, s34, 0x6400\n"
    "s_nop 0\n"
    "buffer_load_dwordx4 v171, s[28:31], s67 offen lds\n"
    "s_add_u32 s67, s67, 0x30000\n"
    "s_add_u32 m0, s34, 0x6800\n"
    "s_nop 0\n"
    "buffer_load_dwordx4 v170, s[28:31], s68 offen lds\n"
    "s_add_u32 s68, s68, 0x30000\n"
    "s_add_u32 m0, s34, 0x6c00\n"
    "s_nop 0\n"
    "buffer_load_dwordx4 v171, s[28:31], s69 offen lds\n"
    "s_add_u32 s69, s69, 0x30000\n"
    "s_add_u32 m0, s34, 0x7000\n"
    "s_nop 0\n"
    "buffer_load_dwordx4 v170, s[28:31], s70 offen lds\n"
    "s_add_u32 s70, s70, 0x30000\n"
    "s_add_u32 m0, s34, 0x7400\n"
    "s_nop 0\n"
    "buffer_load_dwordx4 v171, s[28:31], s71 offen lds\n"
    "s_add_u32 s71, s71, 0x30000\n"
    "s_waitcnt vmcnt(6)\n"
    "s_barrier\n"
    "ds_read_b64_tr_b16 v[120:121], v10 offset:8192\n"
    "ds_read_b64_tr_b16 v[122:123], v10 offset:8704\n"
    "ds_read_b64_tr_b16 v[124:125], v10 offset:16384\n"
    "ds_read_b64_tr_b16 v[126:127], v10 offset:16896\n"
    "ds_read_b64_tr_b16 v[116:117], v10\n"
    "ds_read_b64_tr_b16 v[118:119], v10 offset:512\n"
    "ds_read_b128 v[92:95], v12\n"
    "ds_read_b128 v[96:99], v12 offset:8192\n"
    "ds_read_b128 v[100:103], v12 offset:16384\n"
    "ds_read_b128 v[140:143], v16\n"
    "ds_read_b128 v[144:147], v17\n"
    "s_waitcnt lgkmcnt(1)\n"
    "v_mfma_f32_16x16x32_bf16 v[84:87], v[140:143], v[140:143], 0\n"
    "s_add_u32 m0, s34, 0xc000\n"
    "s_nop 0\n"
    "buffer_load_dwordx4 v170, s[28:31], s66 offen lds\n"
    "ds_read_b128 v[104:107], v13\n"
    "v_mfma_f32_16x16x32_bf16 v[88:91], v[32:35], v[140:143], 0\n"
    "ds_read_b128 v[108:111], v13 offset:8192\n"
    "s_waitcnt lgkmcnt(2)\n"
    "v_mfma_f32_16x16x32_bf16 v[84:87], v[144:147], v[144:147], v[84:87]\n"
    "s_add_u32 s66, s66, 0x30000\n"
    "s_add_u32 m0, s34, 0xc400\n"
    "ds_read_b128 v[112:115], v13 offset:16384\n"
    "v_mfma_f32_16x16x32_bf16 v[88:91], v[32:35], v[144:147], v[88:91]\n"
    "ds_read_b64_tr_b16 v[132:133], v10 offset:12288\n"
    "v_cndmask_b32 v92, v92, v32, s[74:75]\n"
    "v_cndmask_b32 v93, v93, v32, s[74:75]\n"
    "v_cndmask_b32 v94, v94, v32, s[74:75]\n"
    "v_cndmask_b32 v95, v95, v32, s[74:75]\n"
    "v_mfma_f32_16x16x32_bf16 v[44:47], v[120:123], v[96:99], 0\n"
    "buffer_load_dwordx4 v171, s[28:31], s67 offen lds\n"
    "s_add_u32 s67, s67, 0x30000\n"
    "ds_read_b64_tr_b16 v[134:135], v10 offset:12800\n"
    "v_mfma_f32_16x16x32_bf16 v[56:59], v[96:99], v[124:127], 0\n"
    "ds_read_b64_tr_b16 v[136:137], v10 offset:20480\n"
    "v_mfma_f32_16x16x32_bf16 v[64:67], v[116:119], v[96:99], 0\n"
    "s_add_u32 m0, s34, 0xc800\n"
    "s_nop 0\n"
    "buffer_load_dwordx4 v170, s[28:31], s68 offen lds\n"
    "ds_read_b64_tr_b16 v[138:139], v10 offset:20992\n"
    "v_mfma_f32_16x16x32_bf16 v[48:51], v[120:123], v[100:103], 0\n"
    "ds_read_b64_tr_b16 v[128:129], v10 offset:4096\n"
    "v_mfma_f32_16x16x32_bf16 v[60:63], v[100:103], v[124:127], 0\n"
    "s_add_u32 s68, s68, 0x30000\n"
    "s_add_u32 m0, s34, 0xcc00\n"
    "ds_read_b64_tr_b16 v[130:131], v10 offset:4608\n"
    "v_mfma_f32_16x16x32_bf16 v[68:71], v[116:119], v[100:103], 0\n"
    "v_mfma_f32_16x16x32_bf16 v[40:43], v[120:123], v[92:95], 0\n"
    "buffer_load_dwordx4 v171, s[28:31], s69 offen lds\n"
    "s_add_u32 s69, s69, 0x30000\n"
    "v_mfma_f32_16x16x32_bf16 v[52:55], v[92:95], v[124:127], 0\n"
    "v_mfma_f32_16x16x32_bf16 v[72:75], v[116:119], v[32:35], 0\n"
    "s_add_u32 m0, s34, 0xd000\n"
    "s_nop 0\n"
    "buffer_load_dwordx4 v170, s[28:31], s70 offen lds\n"
    "s_waitcnt lgkmcnt(8)\n"
    "v_cndmask_b32 v104, v104, v32, s[74:75]\n"
    "v_cndmask_b32 v105, v105, v32, s[74:75]\n"
    "v_cndmask_b32 v106, v106, v32, s[74:75]\n"
    "v_cndmask_b32 v107, v107, v32, s[74:75]\n"
    "s_waitcnt lgkmcnt(4)\n"
    "v_mfma_f32_16x16x32_bf16 v[44:47], v[132:135], v[108:111], v[44:47]\n"
    "s_waitcnt lgkmcnt(2)\n"
    "v_mfma_f32_16x16x32_bf16 v[56:59], v[108:111], v[136:139], v[56:59]\n"
    "s_add_u32 s70, s70, 0x30000\n"
    "s_add_u32 m0, s34, 0xd400\n"
    "s_waitcnt lgkmcnt(0)\n"
    "v_mfma_f32_16x16x32_bf16 v[64:67], v[128:131], v[108:111], v[64:67]\n"
    "v_mfma_f32_16x16x32_bf16 v[48:51], v[132:135], v[112:115], v[48:51]\n"
    "buffer_load_dwordx4 v171, s[28:31], s71 offen lds\n"
    "s_add_u32 s71, s71, 0x30000\n"
    "v_mfma_f32_16x16x32_bf16 v[60:63], v[112:115], v[136:139], v[60:63]\n"
    "v_mfma_f32_16x16x32_bf16 v[68:71], v[128:131], v[112:115], v[68:71]\n"
    "v_mfma_f32_16x16x32_bf16 v[40:43], v[132:135], v[104:107], v[40:43]\n"
    "v_mfma_f32_16x16x32_bf16 v[52:55], v[104:107], v[136:139], v[52:55]\n"
    "v_mfma_f32_16x16x32_bf16 v[72:75], v[128:131], v[32:35], v[72:75]\n"
    "ds_read_b128 v[92:95], v14\n"
    "ds_read_b128 v[96:99], v14 offset:8192\n"
    "ds_read_b128 v[100:103], v14 offset:16384\n"
    "ds_read_b128 v[148:151], v18\n"
    "ds_read_b128 v[152:155], v19\n"
    "s_waitcnt vmcnt(6)\n"
    "s_barrier\n"
    "ds_read_b64_tr_b16 v[120:121], v10 offset:32768\n"
    "ds_read_b64_tr_b16 v[122:123], v10 offset:33280\n"
    "ds_read_b64_tr_b16 v[124:125], v10 offset:40960\n"
    "ds_read_b64_tr_b16 v[126:127], v10 offset:41472\n"
    "ds_read_b64_tr_b16 v[116:117], v10 offset:24576\n"
    "ds_read_b64_tr_b16 v[118:119], v10 offset:25088\n"
    "s_waitcnt lgkmcnt(7)\n"
    "v_mfma_f32_16x16x32_bf16 v[84:87], v[148:151], v[148:151], v[84:87]\n"
    "s_add_u32 m0, s34, 0\n"
    "s_nop 0\n"
    "buffer_load_dwordx4 v170, s[28:31], s66 offen lds\n"
    "ds_read_b128 v[104:107], v15\n"
    "v_mfma_f32_16x16x32_bf16 v[88:91], v[32:35], v[148:151], v[88:91]\n"
    "s_add_u32 s66, s66, 0x30000\n"
    "s_add_u32 m0, s34, 0x400\n"
    "ds_read_b128 v[108:111], v15 offset:8192\n"
    "s_waitcnt lgkmcnt(8)\n"
    "v_mfma_f32_16x16x32_bf16 v[84:87], v[152:155], v[152:155], v[84:87]\n"
    "buffer_load_dwordx4 v171, s[28:31], s67 offen lds\n"
    "s_add_u32 s67, s67, 0x30000\n"
    "ds_read_b128 v[112:115], v15 offset:16384\n"
    "v_mfma_f32_16x16x32_bf16 v[88:91], v[32:35], v[152:155], v[88:91]\n"
    "s_add_u32 m0, s34, 0x800\n"
    "s_nop 0\n"
    "buffer_load_dwordx4 v170, s[28:31], s68 offen lds\n"
    "ds_read_b64_tr_b16 v[132:133], v10 offset:36864\n"
    "v_cndmask_b32 v92, v92, v32, s[74:75]\n"
    "v_cndmask_b32 v93, v93, v32, s[74:75]\n"
    "v_cndmask_b32 v94, v94, v32, s[74:75]\n"
    "v_cndmask_b32 v95, v95, v32, s[74:75]\n"
    "s_waitcnt lgkmcnt(8)\n"
    "v_mfma_f32_16x16x32_bf16 v[44:47], v[120:123], v[96:99], v[44:47]\n"
    "s_add_u32 s68, s68, 0x30000\n"
    "s_add_u32 m0, s34, 0xc00\n"
    "ds_read_b64_tr_b16 v[134:135], v10 offset:37376\n"
    "s_waitcnt lgkmcnt(7)\n"
    "v_mfma_f32_16x16x32_bf16 v[56:59], v[96:99], v[124:127], v[56:59]\n"
    "buffer_load_dwordx4 v171, s[28:31], s69 offen lds\n"
    "s_add_u32 s69, s69, 0x30000\n"
    "ds_read_b64_tr_b16 v[136:137], v10 offset:45056\n"
    "s_waitcnt lgkmcnt(6)\n"
    "v_mfma_f32_16x16x32_bf16 v[64:67], v[116:119], v[96:99], v[64:67]\n"
    "s_add_u32 m0, s34, 0x1000\n"
    "s_nop 0\n"
    "buffer_load_dwordx4 v170, s[28:31], s70 offen lds\n"
    "ds_read_b64_tr_b16 v[138:139], v10 offset:45568\n"
    "v_mfma_f32_16x16x32_bf16 v[48:51], v[120:123], v[100:103], v[48:51]\n"
    "s_add_u32 s70, s70, 0x30000\n"
    "s_add_u32 m0, s34, 0x1400\n"
    "ds_read_b64_tr_b16 v[128:129], v10 offset:28672\n"
    "v_mfma_f32_16x16x32_bf16 v[60:63], v[100:103], v[124:127], v[60:63]\n"
    "buffer_load_dwordx4 v171, s[28:31], s71 offen lds\n"
    "s_add_u32 s71, s71, 0x30000\n"
    "ds_read_b64_tr_b16 v[130:131], v10 offset:29184\n"
    "v_mfma_f32_16x16x32_bf16 v[68:71], v[116:119], v[100:103], v[68:71]\n"
    "s_add_u32 m0, s33, 0x9e00\n"
    "s_nop 0\n"
    "buffer_load_dwordx4 v160, s[24:27], 0 offen offset:512 lds\n"
    "v_mfma_f32_16x16x32_bf16 v[40:43], v[120:123], v[92:95], v[40:43]\n"
    "s_add_u32 m0, s33, 0xa200\n"
    "s_nop 0\n"
    "buffer_load_dwordx4 v161, s[24:27], 0 offen offset:512 lds\n"
    "v_mfma_f32_16x16x32_bf16 v[52:55], v[92:95], v[124:127], v[52:55]\n"
    "s_add_u32 m0, s33, 0xa600\n"
    "s_nop 0\n"
    "buffer_load_dwordx4 v162, s[24:27], 0 offen offset:512 lds\n"
    "v_mfma_f32_16x16x32_bf16 v[72:75], v[116:119], v[32:35], v[72:75]\n"
    "s_add_u32 m0, s33, 0xaa00\n"
    "s_nop 0\n"
    "buffer_load_dwordx4 v163, s[24:27], 0 offen offset:512 lds\n"
    "s_waitcnt lgkmcnt(8)\n"
    "v_cndmask_b32 v104, v104, v32, s[74:75]\n"
    "v_cndmask_b32 v105, v105, v32, s[74:75]\n"
    "v_cndmask_b32 v106, v106, v32, s[74:75]\n"
    "v_cndmask_b32 v107, v107, v32, s[74:75]\n"
    "s_waitcnt lgkmcnt(4)\n"
    "v_mfma_f32_16x16x32_bf16 v[44:47], v[132:135], v[108:111], v[44:47]\n"
    "s_add_u32 m0, s33, 0xae00\n"
    "s_nop 0\n"
    "buffer_load_dwordx4 v164, s[24:27], 0 offen offset:512 lds\n"
    "s_waitcnt lgkmcnt(2)\n"
    "v_mfma_f32_16x16x32_bf16 v[56:59], v[108:111], v[136:139], v[56:59]\n"
    "s_waitcnt lgkmcnt(0)\n"
    "v_mfma_f32_16x16x32_bf16 v[64:67], v[128:131], v[108:111], v[64:67]\n"
    "v_mfma_f32_16x16x32_bf16 v[48:51], v[132:135], v[112:115], v[48:51]\n"
    "v_mfma_f32_16x16x32_bf16 v[60:63], v[112:115], v[136:139], v[60:63]\n"
    "v_mfma_f32_16x16x32_bf16 v[68:71], v[128:131], v[112:115], v[68:71]\n"
    "v_mfma_f32_16x16x32_bf16 v[40:43], v[132:135], v[104:107], v[40:43]\n"
    "v_mfma_f32_16x16x32_bf16 v[52:55], v[104:107], v[136:139], v[52:55]\n"
    "v_mfma_f32_16x16x32_bf16 v[72:75], v[128:131], v[32:35], v[72:75]\n"
    "ds_read_b128 v[92:95], v12 offset:256\n"
    "ds_read_b128 v[96:99], v12 offset:8448\n"
    "ds_read_b128 v[100:103], v12 offset:16640\n"
    "ds_read_b128 v[140:143], v16 offset:256\n"
    "ds_read_b128 v[144:147], v17 offset:256\n"
    "s_waitcnt vmcnt(11)\n"
    "s_barrier\n"
    "ds_read_b64_tr_b16 v[120:121], v11 offset:8192\n"
    "ds_read_b64_tr_b16 v[122:123], v11 offset:8704\n"
    "ds_read_b64_tr_b16 v[124:125], v11 offset:16384\n"
    "ds_read_b64_tr_b16 v[126:127], v11 offset:16896\n"
    "ds_read_b64_tr_b16 v[116:117], v11\n"
    "ds_read_b64_tr_b16 v[118:119], v11 offset:512\n"
    "s_waitcnt lgkmcnt(7)\n"
    "v_mfma_f32_16x16x32_bf16 v[84:87], v[140:143], v[140:143], v[84:87]\n"
    "s_add_u32 m0, s34, 0x6000\n"
    "s_nop 0\n"
    "buffer_load_dwordx4 v170, s[28:31], s66 offen lds\n"
    "ds_read_b128 v[104:107], v13 offset:256\n"
    "v_mfma_f32_16x16x32_bf16 v[88:91], v[32:35], v[140:143], v[88:91]\n"
    "s_add_u32 s66, s66, 0x30000\n"
    "s_add_u32 m0, s34, 0x6400\n"
    "ds_read_b128 v[108:111], v13 offset:8448\n"
    "s_waitcnt lgkmcnt(8)\n"
    "v_mfma_f32_16x16x32_bf16 v[84:87], v[144:147], v[144:147], v[84:87]\n"
    "buffer_load_dwordx4 v171, s[28:31], s67 offen lds\n"
    "s_add_u32 s67, s67, 0x30000\n"
    "ds_read_b128 v[112:115], v13 offset:16640\n"
    "v_mfma_f32_16x16x32_bf16 v[88:91], v[32:35], v[144:147], v[88:91]\n"
    "s_add_u32 m0, s34, 0x6800\n"
    "s_nop 0\n"
    "buffer_load_dwordx4 v170, s[28:31], s68 offen lds\n"
    "ds_read_b64_tr_b16 v[132:133], v11 offset:12288\n"
    "v_cndmask_b32 v92, v92, v32, s[74:75]\n"
    "v_cndmask_b32 v93, v93, v32, s[74:75]\n"
    "v_cndmask_b32 v94, v94, v32, s[74:75]\n"
    "v_cndmask_b32 v95, v95, v32, s[74:75]\n"
    "s_waitcnt lgkmcnt(8)\n"
    "v_mfma_f32_16x16x32_bf16 v[44:47], v[120:123], v[96:99], v[44:47]\n"
    "s_add_u32 s68, s68, 0x30000\n"
    "s_add_u32 m0, s34, 0x6c00\n"
    "ds_read_b64_tr_b16 v[134:135], v11 offset:12800\n"
    "s_waitcnt lgkmcnt(7)\n"
    "v_mfma_f32_16x16x32_bf16 v[56:59], v[96:99], v[124:127], v[56:59]\n"
    "buffer_load_dwordx4 v171, s[28:31], s69 offen lds\n"
    "s_add_u32 s69, s69, 0x30000\n"
    "ds_read_b64_tr_b16 v[136:137], v11 offset:20480\n"
    "s_waitcnt lgkmcnt(6)\n"
    "v_mfma_f32_16x16x32_bf16 v[64:67], v[116:119], v[96:99], v[64:67]\n"
    "s_add_u32 m0, s34, 0x7000\n"
    "s_nop 0\n"
    "buffer_load_dwordx4 v170, s[28:31], s70 offen lds\n"
    "ds_read_b64_tr_b16 v[138:139], v11 offset:20992\n"
    "v_mfma_f32_16x16x32_bf16 v[48:51], v[120:123], v[100:103], v[48:51]\n"
    "s_add_u32 s70, s70, 0x30000\n"
    "s_add_u32 m0, s34, 0x7400\n"
    "ds_read_b64_tr_b16 v[128:129], v11 offset:4096\n"
    "v_mfma_f32_16x16x32_bf16 v[60:63], v[100:103], v[124:127], v[60:63]\n"
    "buffer_load_dwordx4 v171, s[28:31], s71 offen lds\n"
    "s_add_u32 s71, s71, 0x30000\n"
    "ds_read_b64_tr_b16 v[130:131], v11 offset:4608\n"
    "v_mfma_f32_16x16x32_bf16 v[68:71], v[116:119], v[100:103], v[68:71]\n"
    "s_add_u32 m0, s33, 0xb200\n"
    "s_nop 0\n"
    "buffer_load_dwordx4 v165, s[24:27], 0 offen offset:512 lds\n"
    "v_mfma_f32_16x16x32_bf16 v[40:43], v[120:123], v[92:95], v[40:43]\n"
    "s_add_u32 m0, s33, 0xb600\n"
    "s_nop 0\n"
    "buffer_load_dwordx4 v166, s[24:27], 0 offen offset:512 lds\n"
    "v_mfma_f32_16x16x32_bf16 v[52:55], v[92:95], v[124:127], v[52:55]\n"
    "s_add_u32 m0, s33, 0xba00\n"
    "s_nop 0\n"
    "buffer_load_dwordx4 v167, s[24:27], 0 offen offset:512 lds\n"
    "v_mfma_f32_16x16x32_bf16 v[72:75], v[116:119], v[32:35], v[72:75]\n"
    "s_add_u32 m0, s33, 0xbe00\n"
    "s_nop 0\n"
    "buffer_load_dwordx4 v168, s[24:27], 0 offen offset:512 lds\n"
    "s_waitcnt lgkmcnt(8)\n"
    "v_cndmask_b32 v104, v104, v32, s[74:75]\n"
    "v_cndmask_b32 v105, v105, v32, s[74:75]\n"
    "v_cndmask_b32 v106, v106, v32, s[74:75]\n"
    "v_cndmask_b32 v107, v107, v32, s[74:75]\n"
    "s_waitcnt lgkmcnt(4)\n"
    "v_mfma_f32_16x16x32_bf16 v[44:47], v[132:135], v[108:111], v[44:47]\n"
    "s_add_u32 m0, s33, 0xc200\n"
    "s_nop 0\n"
    "buffer_load_dwordx4 v169, s[24:27], 0 offen offset:512 lds\n"
    "s_waitcnt lgkmcnt(2)\n"
    "v_mfma_f32_16x16x32_bf16 v[56:59], v[108:111], v[136:139], v[56:59]\n"
    "s_waitcnt lgkmcnt(0)\n"
    "v_mfma_f32_16x16x32_bf16 v[64:67], v[128:131], v[108:111], v[64:67]\n"
    "v_mfma_f32_16x16x32_bf16 v[48:51], v[132:135], v[112:115], v[48:51]\n"
    "v_mfma_f32_16x16x32_bf16 v[60:63], v[112:115], v[136:139], v[60:63]\n"
    "v_mfma_f32_16x16x32_bf16 v[68:71], v[128:131], v[112:115], v[68:71]\n"
    "v_mfma_f32_16x16x32_bf16 v[40:43], v[132:135], v[104:107], v[40:43]\n"
    "v_mfma_f32_16x16x32_bf16 v[52:55], v[104:107], v[136:139], v[52:55]\n"
    "v_mfma_f32_16x16x32_bf16 v[72:75], v[128:131], v[32:35], v[72:75]\n"
    "ds_read_b128 v[92:95], v14 offset:256\n"
    "ds_read_b128 v[96:99], v14 offset:8448\n"
    "ds_read_b128 v[100:103], v14 offset:16640\n"
    "ds_read_b128 v[148:151], v18 offset:256\n"
    "ds_read_b128 v[152:155], v19 offset:256\n"
    "s_waitcnt vmcnt(16)\n"
    "s_barrier\n"
    "ds_read_b64_tr_b16 v[120:121], v10 offset:8192\n"
    "ds_read_b64_tr_b16 v[122:123], v10 offset:8704\n"
    "ds_read_b64_tr_b16 v[124:125], v10 offset:16384\n"
    "ds_read_b64_tr_b16 v[126:127], v10 offset:16896\n"
    "ds_read_b64_tr_b16 v[116:117], v10\n"
    "ds_read_b64_tr_b16 v[118:119], v10 offset:512\n"
    "s_waitcnt lgkmcnt(7)\n"
    "v_mfma_f32_16x16x32_bf16 v[84:87], v[148:151], v[148:151], v[84:87]\n"
    "s_add_u32 m0, s34, 0xc000\n"
    "s_nop 0\n"
    "buffer_load_dwordx4 v170, s[28:31], s66 offen lds\n"
    "ds_read_b128 v[104:107], v15 offset:256\n"
    "v_mfma_f32_16x16x32_bf16 v[88:91], v[32:35], v[148:151], v[88:91]\n"
    "ds_read_b128 v[108:111], v15 offset:8448\n"
    "s_waitcnt lgkmcnt(8)\n"
    "v_mfma_f32_16x16x32_bf16 v[84:87], v[152:155], v[152:155], v[84:87]\n"
    "s_add_u32 s66, s66, 0x30000\n"
    "s_add_u32 m0, s34, 0xc400\n"
    "ds_read_b128 v[112:115], v15 offset:16640\n"
    "v_mfma_f32_16x16x32_bf16 v[88:91], v[32:35], v[152:155], v[88:91]\n"
    "ds_read_b64_tr_b16 v[132:133], v10 offset:12288\n"
    "v_cndmask_b32 v92, v92, v32, s[74:75]\n"
    "v_cndmask_b32 v93, v93, v32, s[74:75]\n"
    "v_cndmask_b32 v94, v94, v32, s[74:75]\n"
    "v_cndmask_b32 v95, v95, v32, s[74:75]\n"
    "s_waitcnt lgkmcnt(8)\n"
    "v_mfma_f32_16x16x32_bf16 v[44:47], v[120:123], v[96:99], v[44:47]\n"
    "buffer_load_dwordx4 v171, s[28:31], s67 offen lds\n"
    "s_add_u32 s67, s67, 0x30000\n"
    "ds_read_b64_tr_b16 v[134:135], v10 offset:12800\n"
    "s_waitcnt lgkmcnt(7)\n"
    "v_mfma_f32_16x16x32_bf16 v[56:59], v[96:99], v[124:127], v[56:59]\n"
    "ds_read_b64_tr_b16 v[136:137], v10 offset:20480\n"
    "s_waitcnt lgkmcnt(6)\n"
    "v_mfma_f32_16x16x32_bf16 v[64:67], v[116:119], v[96:99], v[64:67]\n"
    "s_add_u32 m0, s34, 0xc800\n"
    "s_nop 0\n"
    "buffer_load_dwordx4 v170, s[28:31], s68 offen lds\n"
    "ds_read_b64_tr_b16 v[138:139], v10 offset:20992\n"
    "v_mfma_f32_16x16x32_bf16 v[48:51], v[120:123], v[100:103], v[48:51]\n"
    "ds_read_b64_tr_b16 v[128:129], v10 offset:4096\n"
    "v_mfma_f32_16x16x32_bf16 v[60:63], v[100:103], v[124:127], v[60:63]\n"
    "s_add_u32 s68, s68, 0x30000\n"
    "s_add_u32 m0, s34, 0xcc00\n"
    "ds_read_b64_tr_b16 v[130:131], v10 offset:4608\n"
    "v_mfma_f32_16x16x32_bf16 v[68:71], v[116:119], v[100:103], v[68:71]\n"
    "v_mfma_f32_16x16x32_bf16 v[40:43], v[120:123], v[92:95], v[40:43]\n"
    "buffer_load_dwordx4 v171, s[28:31], s69 offen lds\n"
    "s_add_u32 s69, s69, 0x30000\n"
    "v_mfma_f32_16x16x32_bf16 v[52:55], v[92:95], v[124:127], v[52:55]\n"
    "v_mfma_f32_16x16x32_bf16 v[72:75], v[116:119], v[32:35], v[72:75]\n"
    "s_add_u32 m0, s34, 0xd000\n"
    "s_nop 0\n"
    "buffer_load_dwordx4 v170, s[28:31], s70 offen lds\n"
    "s_waitcnt lgkmcnt(8)\n"
    "v_cndmask_b32 v104, v104, v32, s[74:75]\n"
    "v_cndmask_b32 v105, v105, v32, s[74:75]\n"
    "v_cndmask_b32 v106, v106, v32, s[74:75]\n"
    "v_cndmask_b32 v107, v107, v32, s[74:75]\n"
    "s_waitcnt lgkmcnt(4)\n"
    "v_mfma_f32_16x16x32_bf16 v[44:47], v[132:135], v[108:111], v[44:47]\n"
    "s_waitcnt lgkmcnt(2)\n"
    "v_mfma_f32_16x16x32_bf16 v[56:59], v[108:111], v[136:139], v[56:59]\n"
    "s_add_u32 s70, s70, 0x30000\n"
    "s_add_u32 m0, s34, 0xd400\n"
    "s_waitcnt lgkmcnt(0)\n"
    "v_mfma_f32_16x16x32_bf16 v[64:67], v[128:131], v[108:111], v[64:67]\n"
    "v_mfma_f32_16x16x32_bf16 v[48:51], v[132:135], v[112:115], v[48:51]\n"
    "buffer_load_dwordx4 v171, s[28:31], s71 offen lds\n"
    "s_add_u32 s71, s71, 0x30000\n"
    "v_mfma_f32_16x16x32_bf16 v[60:63], v[112:115], v[136:139], v[60:63]\n"
    "v_mfma_f32_16x16x32_bf16 v[68:71], v[128:131], v[112:115], v[68:71]\n"
    "v_mfma_f32_16x16x32_bf16 v[40:43], v[132:135], v[104:107], v[40:43]\n"
    "v_mfma_f32_16x16x32_bf16 v[52:55], v[104:107], v[136:139], v[52:55]\n"
    "v_mfma_f32_16x16x32_bf16 v[72:75], v[128:131], v[32:35], v[72:75]\n"
    "s_waitcnt vmcnt(11)\n"
    "s_waitcnt vmcnt(6)\n"
    "s_barrier\n"
    "ds_read_b64_tr_b16 v[120:121], v10 offset:32768\n"
    "ds_read_b64_tr_b16 v[122:123], v10 offset:33280\n"
    "ds_read_b64_tr_b16 v[124:125], v10 offset:40960\n"
    "ds_read_b64_tr_b16 v[126:127], v10 offset:41472\n"
    "ds_read_b64_tr_b16 v[116:117], v10 offset:24576\n"
    "ds_read_b64_tr_b16 v[118:119], v10 offset:25088\n"
    "ds_read_b128 v[92:95], v12 offset:40960\n"
    "ds_read_b128 v[96:99], v12 offset:49152\n"
    "ds_read_b128 v[100:103], v12 offset:57344\n"
    "ds_read_b128 v[140:143], v16 offset:40960\n"
    "ds_read_b128 v[144:147], v17 offset:40960\n"
    "ds_read_b128 v[148:151], v18 offset:40960\n"
    "ds_read_b128 v[152:155], v19 offset:40960\n"
    "s_waitcnt lgkmcnt(3)\n"
    "v_mfma_f32_16x16x32_bf16 v[84:87], v[140:143], v[140:143], v[84:87]\n"
    "s_add_u32 m0, s34, 0\n"
    "s_nop 0\n"
    "buffer_load_dwordx4 v170, s[28:31], s66 offen lds\n"
    "ds_read_b128 v[104:107], v13 offset:40960\n"
    "v_mfma_f32_16x16x32_bf16 v[88:91], v[32:35], v[140:143], v[88:91]\n"
    "ds_read_b128 v[108:111], v13 offset:49152\n"
    "s_waitcnt lgkmcnt(4)\n"
    "v_mfma_f32_16x16x32_bf16 v[84:87], v[144:147], v[144:147], v[84:87]\n"
    "s_add_u32 s66, s66, 0x30000\n"
    "s_add_u32 m0, s34, 0x400\n"
    "ds_read_b128 v[112:115], v13 offset:57344\n"
    "v_mfma_f32_16x16x32_bf16 v[88:91], v[32:35], v[144:147], v[88:91]\n"
    "ds_read_b64_tr_b16 v[132:133], v10 offset:36864\n"
    "s_waitcnt lgkmcnt(5)\n"
    "v_mfma_f32_16x16x32_bf16 v[84:87], v[148:151], v[148:151], v[84:87]\n"
    "buffer_load_dwordx4 v171, s[28:31], s67 offen lds\n"
    "s_add_u32 s67, s67, 0x30000\n"
    "ds_read_b64_tr_b16 v[134:135], v10 offset:37376\n"
    "v_mfma_f32_16x16x32_bf16 v[88:91], v[32:35], v[148:151], v[88:91]\n"
    "ds_read_b64_tr_b16 v[136:137], v10 offset:45056\n"
    "s_waitcnt lgkmcnt(6)\n"
    "v_mfma_f32_16x16x32_bf16 v[84:87], v[152:155], v[152:155], v[84:87]\n"
    "s_add_u32 m0, s34, 0x800\n"
    "s_nop 0\n"
    "buffer_load_dwordx4 v170, s[28:31], s68 offen lds\n"
    "ds_read_b64_tr_b16 v[138:139], v10 offset:45568\n"
    "v_mfma_f32_16x16x32_bf16 v[88:91], v[32:35], v[152:155], v[88:91]\n"
    "ds_read_b64_tr_b16 v[128:129], v10 offset:28672\n"
    "v_cndmask_b32 v92, v92, v32, s[74:75]\n"
    "v_cndmask_b32 v93, v93, v32, s[74:75]\n"
    "v_cndmask_b32 v94, v94, v32, s[74:75]\n"
    "v_cndmask_b32 v95, v95, v32, s[74:75]\n"
    "v_mfma_f32_16x16x32_bf16 v[44:47], v[120:123], v[96:99], v[44:47]\n"
    "s_add_u32 s68, s68, 0x30000\n"
    "s_add_u32 m0, s34, 0xc00\n"
    "ds_read_b64_tr_b16 v[130:131], v10 offset:29184\n"
    "v_mfma_f32_16x16x32_bf16 v[56:59], v[96:99], v[124:127], v[56:59]\n"
    "v_mfma_f32_16x16x32_bf16 v[64:67], v[116:119], v[96:99], v[64:67]\n"
    "buffer_load_dwordx4 v171, s[28:31], s69 offen lds\n"
    "s_add_u32 s69, s69, 0x30000\n"
    "v_mfma_f32_16x16x32_bf16 v[48:51], v[120:123], v[100:103], v[48:51]\n"
    "v_mfma_f32_16x16x32_bf16 v[60:63], v[100:103], v[124:127], v[60:63]\n"
    "s_add_u32 m0, s34, 0x1000\n"
    "s_nop 0\n"
    "buffer_load_dwordx4 v170, s[28:31], s70 offen lds\n"
    "v_mfma_f32_16x16x32_bf16 v[68:71], v[116:119], v[100:103], v[68:71]\n"
    "v_mfma_f32_16x16x32_bf16 v[40:43], v[120:123], v[92:95], v[40:43]\n"
    "s_add_u32 s70, s70, 0x30000\n"
    "s_add_u32 m0, s34, 0x1400\n"
    "v_mfma_f32_16x16x32_bf16 v[52:55], v[92:95], v[124:127], v[52:55]\n"
    "v_mfma_f32_16x16x32_bf16 v[72:75], v[116:119], v[32:35], v[72:75]\n"
    "buffer_load_dwordx4 v171, s[28:31], s71 offen lds\n"
    "s_add_u32 s71, s71, 0x30000\n"
    "s_waitcnt lgkmcnt(8)\n"
    "v_cndmask_b32 v104, v104, v32, s[74:75]\n"
    "v_cndmask_b32 v105, v105, v32, s[74:75]\n"
    "v_cndmask_b32 v106, v106, v32, s[74:75]\n"
    "v_cndmask_b32 v107, v107, v32, s[74:75]\n"
    "s_waitcnt lgkmcnt(4)\n"
    "v_mfma_f32_16x16x32_bf16 v[44:47], v[132:135], v[108:111], v[44:47]\n"
    "s_waitcnt lgkmcnt(2)\n"
    "v_mfma_f32_16x16x32_bf16 v[56:59], v[108:111], v[136:139], v[56:59]\n"
    "s_waitcnt lgkmcnt(0)\n"
    "v_mfma_f32_16x16x32_bf16 v[64:67], v[128:131], v[108:111], v[64:67]\n"
    "v_mfma_f32_16x16x32_bf16 v[48:51], v[132:135], v[112:115], v[48:51]\n"
    "v_mfma_f32_16x16x32_bf16 v[60:63], v[112:115], v[136:139], v[60:63]\n"
    "v_mfma_f32_16x16x32_bf16 v[68:71], v[128:131], v[112:115], v[68:71]\n"
    "v_mfma_f32_16x16x32_bf16 v[40:43], v[132:135], v[104:107], v[40:43]\n"
    "v_mfma_f32_16x16x32_bf16 v[52:55], v[104:107], v[136:139], v[52:55]\n"
    "v_mfma_f32_16x16x32_bf16 v[72:75], v[128:131], v[32:35], v[72:75]\n"
    "ds_read_b128 v[92:95], v14 offset:40960\n"
    "ds_read_b128 v[96:99], v14 offset:49152\n"
    "ds_read_b128 v[100:103], v14 offset:57344\n"
    "ds_read_b128 v[140:143], v16 offset:41216\n"
    "ds_read_b128 v[144:147], v17 offset:41216\n"
    "ds_read_b128 v[148:151], v18 offset:41216\n"
    "ds_read_b128 v[152:155], v19 offset:41216\n"
    "s_waitcnt vmcnt(6)\n"
    "s_barrier\n"
    "ds_read_b64_tr_b16 v[120:121], v11 offset:8192\n"
    "ds_read_b64_tr_b16 v[122:123], v11 offset:8704\n"
    "ds_read_b64_tr_b16 v[124:125], v11 offset:16384\n"
    "ds_read_b64_tr_b16 v[126:127], v11 offset:16896\n"
    "ds_read_b64_tr_b16 v[116:117], v11\n"
    "ds_read_b64_tr_b16 v[118:119], v11 offset:512\n"
    "s_waitcnt lgkmcnt(9)\n"
    "v_mfma_f32_16x16x32_bf16 v[84:87], v[140:143], v[140:143], v[84:87]\n"
    "s_add_u32 m0, s34, 0x6000\n"
    "s_nop 0\n"
    "buffer_load_dwordx4 v170, s[28:31], s66 offen lds\n"
    "ds_read_b128 v[104:107], v15 offset:40960\n"
    "v_mfma_f32_16x16x32_bf16 v[88:91], v[32:35], v[140:143], v[88:91]\n"
    "ds_read_b128 v[108:111], v15 offset:49152\n"
    "s_waitcnt lgkmcnt(10)\n"
    "v_mfma_f32_16x16x32_bf16 v[84:87], v[144:147], v[144:147], v[84:87]\n"
    "s_add_u32 m0, s34, 0x6400\n"
    "s_nop 0\n"
    "buffer_load_dwordx4 v171, s[28:31], s67 offen lds\n"
    "ds_read_b128 v[112:115], v15 offset:57344\n"
    "v_mfma_f32_16x16x32_bf16 v[88:91], v[32:35], v[144:147], v[88:91]\n"
    "ds_read_b64_tr_b16 v[132:133], v11 offset:12288\n"
    "s_waitcnt lgkmcnt(11)\n"
    "v_mfma_f32_16x16x32_bf16 v[84:87], v[148:151], v[148:151], v[84:87]\n"
    "s_add_u32 m0, s34, 0x6800\n"
    "s_nop 0\n"
    "buffer_load_dwordx4 v170, s[28:31], s68 offen lds\n"
    "ds_read_b64_tr_b16 v[134:135], v11 offset:12800\n"
    "v_mfma_f32_16x16x32_bf16 v[88:91], v[32:35], v[148:151], v[88:91]\n"
    "ds_read_b64_tr_b16 v[136:137], v11 offset:20480\n"
    "s_waitcnt lgkmcnt(12)\n"
    "v_mfma_f32_16x16x32_bf16 v[84:87], v[152:155], v[152:155], v[84:87]\n"
    "s_add_u32 m0, s34, 0x6c00\n"
    "s_nop 0\n"
    "buffer_load_dwordx4 v171, s[28:31], s69 offen lds\n"
    "ds_read_b64_tr_b16 v[138:139], v11 offset:20992\n"
    "v_mfma_f32_16x16x32_bf16 v[88:91], v[32:35], v[152:155], v[88:91]\n"
    "ds_read_b64_tr_b16 v[128:129], v11 offset:4096\n"
    "v_cndmask_b32 v92, v92, v32, s[74:75]\n"
    "v_cndmask_b32 v93, v93, v32, s[74:75]\n"
    "v_cndmask_b32 v94, v94, v32, s[74:75]\n"
    "v_cndmask_b32 v95, v95, v32, s[74:75]\n"
    "s_waitcnt lgkmcnt(12)\n"
    "v_mfma_f32_16x16x32_bf16 v[44:47], v[120:123], v[96:99], v[44:47]\n"
    "s_add_u32 m0, s34, 0x7000\n"
    "s_nop 0\n"
    "buffer_load_dwordx4 v170, s[28:31], s70 offen lds\n"
    "ds_read_b64_tr_b16 v[130:131], v11 offset:4608\n"
    "s_waitcnt lgkmcnt(11)\n"
    "v_mfma_f32_16x16x32_bf16 v[56:59], v[96:99], v[124:127], v[56:59]\n"
    "s_waitcnt lgkmcnt(9)\n"
    "v_mfma_f32_16x16x32_bf16 v[64:67], v[116:119], v[96:99], v[64:67]\n"
    "s_add_u32 m0, s34, 0x7400\n"
    "s_nop 0\n"
    "buffer_load_dwordx4 v171, s[28:31], s71 offen lds\n"
    "v_mfma_f32_16x16x32_bf16 v[48:51], v[120:123], v[100:103], v[48:51]\n"
    "v_mfma_f32_16x16x32_bf16 v[60:63], v[100:103], v[124:127], v[60:63]\n"
    "v_mfma_f32_16x16x32_bf16 v[68:71], v[116:119], v[100:103], v[68:71]\n"
    "v_mfma_f32_16x16x32_bf16 v[40:43], v[120:123], v[92:95], v[40:43]\n"
    "v_mfma_f32_16x16x32_bf16 v[52:55], v[92:95], v[124:127], v[52:55]\n"
    "v_mfma_f32_16x16x32_bf16 v[72:75], v[116:119], v[32:35], v[72:75]\n"
    "s_waitcnt lgkmcnt(8)\n"
    "v_cndmask_b32 v104, v104, v32, s[74:75]\n"
    "v_cndmask_b32 v105, v105, v32, s[74:75]\n"
    "v_cndmask_b32 v106, v106, v32, s[74:75]\n"
    "v_cndmask_b32 v107, v107, v32, s[74:75]\n"
    "s_waitcnt lgkmcnt(4)\n"
    "v_mfma_f32_16x16x32_bf16 v[44:47], v[132:135], v[108:111], v[44:47]\n"
    "s_waitcnt lgkmcnt(2)\n"
    "v_mfma_f32_16x16x32_bf16 v[56:59], v[108:111], v[136:139], v[56:59]\n"
    "s_waitcnt lgkmcnt(0)\n"
    "v_mfma_f32_16x16x32_bf16 v[64:67], v[128:131], v[108:111], v[64:67]\n"
    "v_mfma_f32_16x16x32_bf16 v[48:51], v[132:135], v[112:115], v[48:51]\n"
    "v_mfma_f32_16x16x32_bf16 v[60:63], v[112:115], v[136:139], v[60:63]\n"
    "v_mfma_f32_16x16x32_bf16 v[68:71], v[128:131], v[112:115], v[68:71]\n"
    "v_mfma_f32_16x16x32_bf16 v[40:43], v[132:135], v[104:107], v[40:43]\n"
    "v_mfma_f32_16x16x32_bf16 v[52:55], v[104:107], v[136:139], v[52:55]\n"
    "v_mfma_f32_16x16x32_bf16 v[72:75], v[128:131], v[32:35], v[72:75]\n"
    "ds_read_b128 v[92:95], v12 offset:41216\n"
    "ds_read_b128 v[96:99], v12 offset:49408\n"
    "ds_read_b128 v[100:103], v12 offset:57600\n"
    "s_waitcnt vmcnt(6)\n"
    "s_barrier\n"
    "ds_read_b64_tr_b16 v[120:121], v10 offset:8192\n"
    "ds_read_b64_tr_b16 v[122:123], v10 offset:8704\n"
    "ds_read_b64_tr_b16 v[124:125], v10 offset:16384\n"
    "ds_read_b64_tr_b16 v[126:127], v10 offset:16896\n"
    "ds_read_b64_tr_b16 v[116:117], v10\n"
    "ds_read_b64_tr_b16 v[118:119], v10 offset:512\n"
    "s_waitcnt lgkmcnt(8)\n"
    "v_cndmask_b32 v92, v92, v32, s[74:75]\n"
    "v_cndmask_b32 v93, v93, v32, s[74:75]\n"
    "v_cndmask_b32 v94, v94, v32, s[74:75]\n"
    "v_cndmask_b32 v95, v95, v32, s[74:75]\n"
    "s_waitcnt lgkmcnt(4)\n"
    "v_mfma_f32_16x16x32_bf16 v[44:47], v[120:123], v[96:99], v[44:47]\n"
    "v_and_b32 v22, 3, v3\n"
    "v_cmp_eq_u32 vcc, 1, v22\n"
    "s_nop 1\n"
    "v_cndmask_b32 v23, v84, v85, vcc\n"
    "ds_read_b128 v[104:107], v13 offset:41216\n"
    "s_waitcnt lgkmcnt(3)\n"
    "v_mfma_f32_16x16x32_bf16 v[56:59], v[96:99], v[124:127], v[56:59]\n"
    "v_cmp_eq_u32 vcc, 2, v22\n"
    "s_nop 1\n"
    "v_cndmask_b32 v23, v23, v86, vcc\n"
    "v_cmp_eq_u32 vcc, 3, v22\n"
    "s_nop 1\n"
    "v_cndmask_b32 v23, v23, v87, vcc\n"
    "ds_read_b128 v[108:111], v13 offset:49408\n"
    "s_waitcnt lgkmcnt(2)\n"
    "v_mfma_f32_16x16x32_bf16 v[64:67], v[116:119], v[96:99], v[64:67]\n"
    "v_mul_f32 v26, 0x3b000000, v88\n"
    "v_mul_f32 v23, 0x3b000000, v23\n"
    "ds_read_b128 v[112:115], v13 offset:57600\n"
    "v_mfma_f32_16x16x32_bf16 v[48:51], v[120:123], v[100:103], v[48:51]\n"
    "v_fma_f32 v23, -v26, v26, v23\n"
    "v_max_f32 v23, 0, v23\n"
    "ds_read_b64_tr_b16 v[132:133], v10 offset:12288\n"
    "v_mfma_f32_16x16x32_bf16 v[60:63], v[100:103], v[124:127], v[60:63]\n"
    "v_add_f32 v23, 0x3727c5ac, v23\n"
    "v_rsq_f32 v27, v23\n"
    "ds_read_b64_tr_b16 v[134:135], v10 offset:12800\n"
    "v_mfma_f32_16x16x32_bf16 v[68:71], v[116:119], v[100:103], v[68:71]\n"
    "s_add_u32 s60, s53, 1\n"
    "s_mul_i32 s60, s60, s57\n"
    "ds_read_b64_tr_b16 v[136:137], v10 offset:20480\n"
    "v_mfma_f32_16x16x32_bf16 v[40:43], v[120:123], v[92:95], v[40:43]\n"
    "s_lshl_b32 s60, s60, 4\n"
    "v_add_u32 v20, s60, v3\n"
    "ds_read_b64_tr_b16 v[138:139], v10 offset:20992\n"
    "v_mfma_f32_16x16x32_bf16 v[52:55], v[92:95], v[124:127], v[52:55]\n"
    "v_lshrrev_b32 v22, 2, v3\n"
    "v_cmp_eq_u32 vcc, v22, v2\n"
    "s_nop 1\n"
    "v_cndmask_b32 v20, v32, v20, vcc\n"
    "ds_read_b64_tr_b16 v[128:129], v10 offset:4096\n"
    "v_mfma_f32_16x16x32_bf16 v[72:75], v[116:119], v[32:35], v[72:75]\n"
    "v_min_u32 v20, 0xff, v20\n"
    "v_lshlrev_b32 v21, 2, v20\n"
    "ds_read_b64_tr_b16 v[130:131], v10 offset:4608\n"
    "s_waitcnt lgkmcnt(8)\n"
    "v_cndmask_b32 v104, v104, v32, s[74:75]\n"
    "v_cndmask_b32 v105, v105, v32, s[74:75]\n"
    "v_cndmask_b32 v106, v106, v32, s[74:75]\n"
    "v_cndmask_b32 v107, v107, v32, s[74:75]\n"
    "s_waitcnt lgkmcnt(4)\n"
    "v_mfma_f32_16x16x32_bf16 v[44:47], v[132:135], v[108:111], v[44:47]\n"
    "v_lshlrev_b32 v20, 3, v20\n"
    "v_add_u32 v20, 0x26000, v20\n"
    "s_waitcnt lgkmcnt(2)\n"
    "v_mfma_f32_16x16x32_bf16 v[56:59], v[108:111], v[136:139], v[56:59]\n"
    "v_add_u32 v21, 0x26800, v21\n"
    "ds_write_b64 v20, v[26:27]\n"
    "s_waitcnt lgkmcnt(1)\n"
    "v_mfma_f32_16x16x32_bf16 v[64:67], v[128:131], v[108:111], v[64:67]\n"
    "ds_write_b32 v21, v26\n"
    "ds_write_b32 v21, v27 offset:1024\n"
    "v_mfma_f32_16x16x32_bf16 v[48:51], v[132:135], v[112:115], v[48:51]\n"
    "v_mfma_f32_16x16x32_bf16 v[60:63], v[112:115], v[136:139], v[60:63]\n"
    "v_mfma_f32_16x16x32_bf16 v[68:71], v[128:131], v[112:115], v[68:71]\n"
    "v_mfma_f32_16x16x32_bf16 v[40:43], v[132:135], v[104:107], v[40:43]\n"
    "v_mfma_f32_16x16x32_bf16 v[52:55], v[104:107], v[136:139], v[52:55]\n"
    "v_mfma_f32_16x16x32_bf16 v[72:75], v[128:131], v[32:35], v[72:75]\n"
    "s_waitcnt vmcnt(0)\n"
    "s_nop 6\n"
    "s_branch .Lp1_join\n"
    ".Lp1_b:\n"
    "s_getpc_b64 s[58:59]\n"
    ".Lpcbase:\n"
    "s_and_b32 s60, s48, 0xf8\n"
    "s_lshr_b32 s60, s60, 1\n"
    "s_add_u32 s60, s60, s53\n"
    "s_lshl_b32 s60, s60, 8\n"
    "v_lshlrev_b32 v20, 2, v0\n"
    "v_add_u32 v20, s60, v20\n"
    "v_min_u32 v20, .Lend-.Lpcbase-4, v20\n"
    "global_load_dword v31, v20, s[58:59]\n"
    "s_mov_b32 s76, 1\n"
    "s_mov_b32 s77, s48\n"
    "s_add_u32 s60, s48, 0x100\n"
    "s_sub_u32 s61, s48, 0x60\n"
    "s_cmp_lt_u32 s48, 0x60\n"
    "s_cselect_b32 s60, s60, s61\n"
    "s_cselect_b32 s61, 1, 2\n"
    "s_cmp_eq_u32 s53, 1\n"
    "s_cselect_b32 s77, s60, s77\n"
    "s_cselect_b32 s76, s61, s76\n"
    "s_add_u32 s60, s48, 0xa0\n"
    "s_cmp_eq_u32 s53, 2\n"
    "s_cselect_b32 s77, s60, s77\n"
    "s_cselect_b32 s76, 2, s76\n"
    "s_add_u32 s60, s48, 0x1a0\n"
    "s_cmp_lt_u32 s48, 56\n"
    "s_cselect_b32 s61, 2, 0\n"
    "s_cmp_eq_u32 s53, 3\n"
    "s_cselect_b32 s77, s60, s77\n"
    "s_cselect_b32 s76, s61, s76\n"
    "s_cmp_eq_u32 s76, 1\n"
    "s_cbranch_scc0 .Lpre_a\n"
    "v_mul_u32_u24 v26, 0xac0, v24\n"
    "v_add_u32 v26, v26, v25\n"
    "s_and_b32 s60, s77, 15\n"
    "s_mul_i32 s60, s60, 0x15800\n"
    "s_lshr_b32 s61, s77, 4\n"
    "s_lshl_b32 s61, s61, 7\n"
    "s_add_u32 s60, s60, s61\n"
    "s_waitcnt lgkmcnt(0)\n"
    "s_mov_b32 s44, s14\n"
    "s_and_b32 s45, s15, 0xffff\n"
    "s_mov_b32 s46, 0x158000\n"
    "s_mov_b32 s47, 0x20000\n"
    "s_mov_b32 s24, s16\n"
    "s_and_b32 s25, s17, 0xffff\n"
    "s_mov_b32 s26, 0x158000\n"
    "s_mov_b32 s27, 0x20000\n"
    "buffer_load_dwordx4 v[176:179], v26, s[44:47], s60 offen\n"
    "buffer_load_dwordx4 v[192:195], v26, s[24:27], s60 offen\n"
    "s_add_u32 s60, s60, 0x5600\n"
    "buffer_load_dwordx4 v[180:183], v26, s[44:47], s60 offen\n"
    "buffer_load_dwordx4 v[196:199], v26, s[24:27], s60 offen\n"
    "s_add_u32 s60, s60, 0x5600\n"
    "buffer_load_dwordx4 v[184:187], v26, s[44:47], s60 offen\n"
    "buffer_load_dwordx4 v[200:203], v26, s[24:27], s60 offen\n"
    "s_add_u32 s60, s60, 0x5600\n"
    "buffer_load_dwordx4 v[188:191], v26, s[44:47], s60 offen\n"
    "buffer_load_dwordx4 v[204:207], v26, s[24:27], s60 offen\n"
    ".Lpre_a:\n"
    "s_cmp_eq_u32 s76, 2\n"
    "s_cbranch_scc0 .Lpre_b\n"
    "v_lshl_add_u32 v26, v24, 10, v25\n"
    "s_cmp_lt_u32 s77, 0x80\n"
    "s_cselect_b32 s61, 0, 0x80\n"
    "s_mov_b32 s46, 0x158000\n"
    "s_cselect_b32 s46, 0x80000, s46\n"
    "s_mov_b32 s47, 0x20000\n"
    "s_waitcnt lgkmcnt(0)\n"
    "s_cselect_b32 s44, s12, s18\n"
    "s_cselect_b32 s45, s13, s19\n"
    "s_and_b32 s45, s45, 0xffff\n"
    "s_sub_u32 s61, s77, s61\n"
    "s_and_b32 s60, s61, 7\n"
    "s_lshl_b32 s60, s60, 7\n"
    "s_lshr_b32 s61, s61, 3\n"
    "s_lshl_b32 s61, s61, 15\n"
    "s_add_u32 s60, s60, s61\n"
    "buffer_load_dwordx4 v[208:211], v26, s[44:47], s60 offen\n"
    "s_add_u32 s60, s60, 0x2000\n"
    "buffer_load_dwordx4 v[212:215], v26, s[44:47], s60 offen\n"
    "s_add_u32 s60, s60, 0x2000\n"
    "buffer_load_dwordx4 v[216:219], v26, s[44:47], s60 offen\n"
    "s_add_u32 s60, s60, 0x2000\n"
    "buffer_load_dwordx4 v[220:223], v26, s[44:47], s60 offen\n"
    ".Lpre_b:\n"
    "s_waitcnt lgkmcnt(0)\n"
    "s_mov_b32 s36, s22\n"
    "s_and_b32 s37, s23, 0xffff\n"
    "s_mov_b32 s38, 0xe89000\n"
    "s_mov_b32 s39, 0x20000\n"
    "s_add_u32 s58, s22, 0xe88000\n"
    "s_addc_u32 s59, s23, 0\n"
    "s_load_dword s56, s[58:59], 0x0\n"
    "s_waitcnt lgkmcnt(0)\n"
    "s_mov_b32 s40, s20\n"
    "s_and_b32 s41, s21, 0xffff\n"
    "s_mov_b32 s42, 0x200000\n"
    "s_mov_b32 s43, 0x20000\n"
    "s_and_b32 s58, s48, 7\n"
    "s_lshr_b32 s59, s48, 3\n"
    "s_lshr_b32 s52, s58, 2\n"
    "s_lshl_b32 s52, s52, 3\n"
    "s_lshr_b32 s60, s59, 2\n"
    "s_add_u32 s52, s52, s60\n"
    "s_and_b32 s62, s58, 3\n"
    "s_lshl_b32 s62, s62, 2\n"
    "s_and_b32 s60, s59, 3\n"
    "s_add_u32 s62, s62, s60\n"
    "s_mul_i32 s54, s62, 11\n"
    "s_sub_i32 s60, s62, 12\n"
    "s_max_i32 s60, s60, 0\n"
    "s_sub_u32 s54, s54, s60\n"
    "s_mul_i32 s60, s53, 3\n"
    "s_add_u32 s54, s54, s60\n"
    "s_cmp_ge_u32 s62, 12\n"
    "s_cselect_b32 s61, 1, 0\n"
    "s_cmp_eq_u32 s53, 3\n"
    "s_cselect_b32 s63, s61, 0\n"
    "s_sub_u32 s54, s54, s63\n"
    "s_add_u32 s63, s53, s61\n"
    "s_cmp_ge_u32 s63, 3\n"
    "s_cselect_b32 s55, 2, 3\n"
    "v_mov_b32 v32, 0x3f803f80\n"
    "v_mov_b32 v33, 0x3f803f80\n"
    "v_mov_b32 v34, 0x3f803f80\n"
    "v_mov_b32 v35, 0x3f803f80\n"
    "s_mov_b32 s74, 0x10001\n"
    "s_mov_b32 s75, 0x10001\n"
    "s_lshl_b32 s60, s57, 14\n"
    "s_add_u32 s61, s53, 1\n"
    "s_mul_i32 s61, s61, s57\n"
    "s_lshl_b32 s61, s61, 13\n"
    "v_xor_b32 v20, v2, v3\n"
    "v_lshlrev_b32 v20, 4, v20\n"
    "v_lshl_add_u32 v20, v3, 9, v20\n"
    "v_xor_b32 v21, 0, v20\n"
    "v_add_u32 v12, s60, v21\n"
    "v_add_u32 v16, s61, v21\n"
    "v_xor_b32 v21, 64, v20\n"
    "v_add_u32 v13, s60, v21\n"
    "v_add_u32 v17, s61, v21\n"
    "v_xor_b32 v21, 0x80, v20\n"
    "v_add_u32 v14, s60, v21\n"
    "v_add_u32 v18, s61, v21\n"
    "v_xor_b32 v21, 0xc0, v20\n"
    "v_add_u32 v15, s60, v21\n"
    "v_add_u32 v19, s61, v21\n"
    "v_bfe_u32 v27, v0, 2, 2\n"
    "v_and_b32 v28, 3, v0\n"
    "v_lshrrev_b32 v20, 1, v28\n"
    "s_lshl_b32 s60, s53, 1\n"
    "v_add_u32 v20, s60, v20\n"
    "v_and_b32 v21, 2, v27\n"
    "v_xor_b32 v20, v20, v21\n"
    "v_and_b32 v21, 1, v2\n"
    "v_lshlrev_b32 v21, 2, v21\n"
    "v_xor_b32 v20, v20, v21\n"
    "v_lshlrev_b32 v20, 4, v20\n"
    "v_and_b32 v21, 1, v28\n"
    "v_lshl_add_u32 v20, v21, 3, v20\n"
    "v_lshl_add_u32 v29, v2, 3, v27\n"
    "v_lshl_add_u32 v10, v29, 7, v20\n"
    "v_add_u32 v10, 0x14000, v10\n"
    "v_add_u32 v11, 0xc000, v10\n"
    "s_add_u32 s56, s56, 1\n"
    "s_barrier\n"
    "ds_read_b64_tr_b16 v[120:121], v10 offset:8192\n"
    "ds_read_b64_tr_b16 v[122:123], v10 offset:8704\n"
    "ds_read_b64_tr_b16 v[124:125], v10 offset:16384\n"
    "ds_read_b64_tr_b16 v[126:127], v10 offset:16896\n"
    "ds_read_b64_tr_b16 v[116:117], v10\n"
    "ds_read_b64_tr_b16 v[118:119], v10 offset:512\n"
    "ds_read_b128 v[92:95], v12\n"
    "ds_read_b128 v[96:99], v12 offset:8192\n"
    "ds_read_b128 v[100:103], v12 offset:16384\n"
    "ds_read_b128 v[140:143], v16\n"
    "ds_read_b128 v[144:147], v17\n"
    "s_waitcnt lgkmcnt(1)\n"
    "v_mfma_f32_16x16x32_bf16 v[84:87], v[140:143], v[140:143], 0\n"
    "v_mov_b32 v36, -1\n"
    "v_mov_b32 v37, -1\n"
    "ds_read_b128 v[104:107], v13\n"
    "v_mfma_f32_16x16x32_bf16 v[88:91], v[32:35], v[140:143], 0\n"
    "v_mov_b32 v38, -1\n"
    "v_mov_b32 v39, -1\n"
    "ds_read_b128 v[108:111], v13 offset:8192\n"
    "s_waitcnt lgkmcnt(2)\n"
    "v_mfma_f32_16x16x32_bf16 v[84:87], v[144:147], v[144:147], v[84:87]\n"
    "s_lshl_b32 s64, s50, 3\n"
    "s_add_u32 s64, s64, s51\n"
    "ds_read_b128 v[112:115], v13 offset:16384\n"
    "v_mfma_f32_16x16x32_bf16 v[88:91], v[32:35], v[144:147], v[88:91]\n"
    "s_lshl_b32 s64, s64, 2\n"
    "s_add_u32 s64, s64, s53\n"
    "ds_read_b64_tr_b16 v[132:133], v10 offset:12288\n"
    "v_cndmask_b32 v92, v92, v32, s[74:75]\n"
    "v_cndmask_b32 v93, v93, v32, s[74:75]\n"
    "v_cndmask_b32 v94, v94, v32, s[74:75]\n"
    "v_cndmask_b32 v95, v95, v32, s[74:75]\n"
    "v_mfma_f32_16x16x32_bf16 v[44:47], v[120:123], v[96:99], 0\n"
    "s_lshl_b32 s66, s64, 11\n"
    "s_add_u32 s66, s66, 0x488000\n"
    "ds_read_b64_tr_b16 v[134:135], v10 offset:12800\n"
    "v_mfma_f32_16x16x32_bf16 v[56:59], v[96:99], v[124:127], 0\n"
    "s_add_u32 s67, s66, 0x200000\n"
    "s_mul_i32 s68, s64, 0x1580\n"
    "ds_read_b64_tr_b16 v[136:137], v10 offset:20480\n"
    "v_mfma_f32_16x16x32_bf16 v[64:67], v[116:119], v[96:99], 0\n"
    "s_add_u32 s68, s68, 0x888000\n"
    "s_add_u32 s69, s68, 0x1000\n"
    "ds_read_b64_tr_b16 v[138:139], v10 offset:20992\n"
    "v_mfma_f32_16x16x32_bf16 v[48:51], v[120:123], v[100:103], 0\n"
    "s_lshl_b32 s70, s64, 8\n"
    "s_add_u32 s70, s70, 0xde8000\n"
    "ds_read_b64_tr_b16 v[128:129], v10 offset:4096\n"
    "v_mfma_f32_16x16x32_bf16 v[60:63], v[100:103], v[124:127], 0\n"
    "buffer_store_dwordx4 v[36:39], v1, s[36:39], s66 offen sc1\n"
    "buffer_store_dwordx4 v[36:39], v1, s[36:39], s66 offen offset:1024 sc1\n"
    "ds_read_b64_tr_b16 v[130:131], v10 offset:4608\n"
    "v_mfma_f32_16x16x32_bf16 v[68:71], v[116:119], v[100:103], 0\n"
    "buffer_store_dwordx4 v[36:39], v1, s[36:39], s67 offen sc1\n"
    "buffer_store_dwordx4 v[36:39], v1, s[36:39], s67 offen offset:1024 sc1\n"
    "v_mfma_f32_16x16x32_bf16 v[40:43], v[120:123], v[92:95], 0\n"
    "buffer_store_dwordx4 v[36:39], v1, s[36:39], s70 offen sc1\n"
    "v_mfma_f32_16x16x32_bf16 v[52:55], v[92:95], v[124:127], 0\n"
    "v_mfma_f32_16x16x32_bf16 v[72:75], v[116:119], v[32:35], 0\n"
    "s_waitcnt lgkmcnt(8)\n"
    "v_cndmask_b32 v104, v104, v32, s[74:75]\n"
    "v_cndmask_b32 v105, v105, v32, s[74:75]\n"
    "v_cndmask_b32 v106, v106, v32, s[74:75]\n"
    "v_cndmask_b32 v107, v107, v32, s[74:75]\n"
    "s_waitcnt lgkmcnt(4)\n"
    "v_mfma_f32_16x16x32_bf16 v[44:47], v[132:135], v[108:111], v[44:47]\n"
    "s_waitcnt lgkmcnt(2)\n"
    "v_mfma_f32_16x16x32_bf16 v[56:59], v[108:111], v[136:139], v[56:59]\n"
    "s_waitcnt lgkmcnt(0)\n"
    "v_mfma_f32_16x16x32_bf16 v[64:67], v[128:131], v[108:111], v[64:67]\n"
    "v_mfma_f32_16x16x32_bf16 v[48:51], v[132:135], v[112:115], v[48:51]\n"
    "v_mfma_f32_16x16x32_bf16 v[60:63], v[112:115], v[136:139], v[60:63]\n"
    "v_mfma_f32_16x16x32_bf16 v[68:71], v[128:131], v[112:115], v[68:71]\n"
    "v_mfma_f32_16x16x32_bf16 v[40:43], v[132:135], v[104:107], v[40:43]\n"
    "v_mfma_f32_16x16x32_bf16 v[52:55], v[104:107], v[136:139], v[52:55]\n"
    "v_mfma_f32_16x16x32_bf16 v[72:75], v[128:131], v[32:35], v[72:75]\n"
    "s_lshl_b32 s62, s53, 13\n"
    "s_add_u32 s62, s62, 0xa000\n"
    "s_cmp_eq_u32 s76, 1\n"
    "s_nop 4\n"
    "s_cbranch_scc0 .Lpre_c\n"
    "v_lshlrev_b32 v20, 8, v24\n"
    "v_lshl_add_u32 v20, v25, 1, v20\n"
    "v_add_u32 v20, s62, v20\n"
    "s_waitcnt vmcnt(5)\n"
    "ds_write2_b32 v20, v176, v192 offset0:0 offset1:1\n"
    "ds_write2_b32 v20, v177, v193 offset0:2 offset1:3\n"
    "ds_write2_b32 v20, v178, v194 offset0:4 offset1:5\n"
    "ds_write2_b32 v20, v179, v195 offset0:6 offset1:7\n"
    "v_add_u32 v20, 0x800, v20\n"
    "s_waitcnt vmcnt(5)\n"
    "ds_write2_b32 v20, v180, v196 offset0:0 offset1:1\n"
    "ds_write2_b32 v20, v181, v197 offset0:2 offset1:3\n"
    "ds_write2_b32 v20, v182, v198 offset0:4 offset1:5\n"
    "ds_write2_b32 v20, v183, v199 offset0:6 offset1:7\n"
    "v_add_u32 v20, 0x800, v20\n"
    "s_waitcnt vmcnt(5)\n"
    "ds_write2_b32 v20, v184, v200 offset0:0 offset1:1\n"
    "ds_write2_b32 v20, v185, v201 offset0:2 offset1:3\n"
    "ds_write2_b32 v20, v186, v202 offset0:4 offset1:5\n"
    "ds_write2_b32 v20, v187, v203 offset0:6 offset1:7\n"
    "v_add_u32 v20, 0x800, v20\n"
    "s_waitcnt vmcnt(5)\n"
    "ds_write2_b32 v20, v188, v204 offset0:0 offset1:1\n"
    "ds_write2_b32 v20, v189, v205 offset0:2 offset1:3\n"
    "ds_write2_b32 v20, v190, v206 offset0:4 offset1:5\n"
    "ds_write2_b32 v20, v191, v207 offset0:6 offset1:7\n"
    "v_lshlrev_b32 v20, 8, v29\n"
    "v_lshl_add_u32 v20, v28, 3, v20\n"
    "v_add_u32 v20, s62, v20\n"
    "ds_read_b64_tr_b16 v[176:177], v20\n"
    "ds_read_b64_tr_b16 v[178:179], v20 offset:1024\n"
    "ds_read_b64_tr_b16 v[180:181], v20 offset:32\n"
    "ds_read_b64_tr_b16 v[182:183], v20 offset:1056\n"
    "ds_read_b64_tr_b16 v[184:185], v20 offset:64\n"
    "ds_read_b64_tr_b16 v[186:187], v20 offset:1088\n"
    "ds_read_b64_tr_b16 v[188:189], v20 offset:96\n"
    "ds_read_b64_tr_b16 v[190:191], v20 offset:1120\n"
    "ds_read_b64_tr_b16 v[192:193], v20 offset:128\n"
    "ds_read_b64_tr_b16 v[194:195], v20 offset:1152\n"
    "ds_read_b64_tr_b16 v[196:197], v20 offset:160\n"
    "ds_read_b64_tr_b16 v[198:199], v20 offset:1184\n"
    "ds_read_b64_tr_b16 v[200:201], v20 offset:192\n"
    "ds_read_b64_tr_b16 v[202:203], v20 offset:1216\n"
    "ds_read_b64_tr_b16 v[204:205], v20 offset:224\n"
    "ds_read_b64_tr_b16 v[206:207], v20 offset:1248\n"
    "s_lshr_b32 s60, s77, 4\n"
    "s_and_b32 s61, s77, 15\n"
    "s_lshl_b32 s63, s60, 17\n"
    "s_lshl_b32 s61, s61, 10\n"
    "s_add_u32 s61, s61, s63\n"
    "s_add_u32 s61, s61, 0x80000\n"
    "s_mov_b32 s63, 0x4000\n"
    "s_cmp_eq_u32 s60, 21\n"
    "s_cselect_b32 s63, 0x4000000, s63\n"
    "s_waitcnt lgkmcnt(0)\n"
    ".Lpre_c:\n"
    "s_cmp_eq_u32 s76, 2\n"
    "s_cbranch_scc0 .Lpre_d\n"
    "v_add_u32 v20, s62, v1\n"
    "s_waitcnt vmcnt(5)\n"
    "ds_write_b128 v20, v[208:211]\n"
    "s_waitcnt vmcnt(5)\n"
    "ds_write_b128 v20, v[212:215] offset:1024\n"
    "s_waitcnt vmcnt(5)\n"
    "ds_write_b128 v20, v[216:219] offset:2048\n"
    "s_waitcnt vmcnt(5)\n"
    "ds_write_b128 v20, v[220:223] offset:3072\n"
    "v_lshlrev_b32 v20, 7, v29\n"
    "v_lshl_add_u32 v20, v28, 4, v20\n"
    "v_add_u32 v20, s62, v20\n"
    "ds_read_b64_tr_b16 v[208:209], v20\n"
    "ds_read_b64_tr_b16 v[210:211], v20 offset:512\n"
    "ds_read_b64_tr_b16 v[212:213], v20 offset:8\n"
    "ds_read_b64_tr_b16 v[214:215], v20 offset:520\n"
    "ds_read_b64_tr_b16 v[216:217], v20 offset:64\n"
    "ds_read_b64_tr_b16 v[218:219], v20 offset:576\n"
    "ds_read_b64_tr_b16 v[220:221], v20 offset:72\n"
    "ds_read_b64_tr_b16 v[222:223], v20 offset:584\n"
    "s_cmp_lt_u32 s77, 0x80\n"
    "s_cselect_b32 s61, 0, 0x80\n"
    "s_cselect_b32 s63, 16, 43\n"
    "s_cselect_b32 s64, 0, 0x330000\n"
    "s_sub_u32 s61, s77, s61\n"
    "s_and_b32 s60, s61, 7\n"
    "s_mul_i32 s60, s60, s63\n"
    "s_lshr_b32 s61, s61, 3\n"
    "s_add_u32 s60, s60, s61\n"
    "s_lshl_b32 s60, s60, 12\n"
    "s_add_u32 s61, s60, s64\n"
    "s_waitcnt lgkmcnt(0)\n"
    ".Lpre_d:\n"
    "s_cmp_eq_u32 s76, 1\n"
    "s_cbranch_scc0 .Lpre_e\n"
    "s_waitcnt lgkmcnt(0)\n"
    "buffer_store_dwordx4 v[176:179], v1, s[36:39], s61 offen sc1\n"
    "s_add_u32 s61, s61, 0x4000\n"
    "s_waitcnt lgkmcnt(0)\n"
    "buffer_store_dwordx4 v[180:183], v1, s[36:39], s61 offen sc1\n"
    "s_add_u32 s61, s61, 0x4000\n"
    "s_waitcnt lgkmcnt(0)\n"
    "buffer_store_dwordx4 v[184:187], v1, s[36:39], s61 offen sc1\n"
    "s_add_u32 s61, s61, 0x4000\n"
    "s_waitcnt lgkmcnt(0)\n"
    "buffer_store_dwordx4 v[188:191], v1, s[36:39], s61 offen sc1\n"
    "s_add_u32 s61, s61, s63\n"
    "s_waitcnt lgkmcnt(0)\n"
    "buffer_store_dwordx4 v[192:195], v1, s[36:39], s61 offen sc1\n"
    "s_add_u32 s61, s61, 0x4000\n"
    "s_waitcnt lgkmcnt(0)\n"
    "buffer_store_dwordx4 v[196:199], v1, s[36:39], s61 offen sc1\n"
    "s_add_u32 s61, s61, 0x4000\n"
    "s_waitcnt lgkmcnt(0)\n"
    "buffer_store_dwordx4 v[200:203], v1, s[36:39], s61 offen sc1\n"
    "s_add_u32 s61, s61, 0x4000\n"
    "s_waitcnt lgkmcnt(0)\n"
    "buffer_store_dwordx4 v[204:207], v1, s[36:39], s61 offen sc1\n"
    ".Lpre_e:\n"
    "s_cmp_eq_u32 s76, 2\n"
    "s_nop 0\n"
    "s_cbranch_scc0 .Lpre_f\n"
    "s_waitcnt lgkmcnt(0)\n"
    "buffer_store_dwordx4 v[208:211], v1, s[36:39], s61 offen sc1\n"
    "s_waitcnt lgkmcnt(0)\n"
    "buffer_store_dwordx4 v[212:215], v1, s[36:39], s61 offen offset:1024 sc1\n"
    "s_waitcnt lgkmcnt(0)\n"
    "buffer_store_dwordx4 v[216:219], v1, s[36:39], s61 offen offset:2048 sc1\n"
    "s_waitcnt lgkmcnt(0)\n"
    "buffer_store_dwordx4 v[220:223], v1, s[36:39], s61 offen offset:3072 sc1\n"
    ".Lpre_f:\n"
    "ds_read_b128 v[92:95], v14\n"
    "ds_read_b128 v[96:99], v14 offset:8192\n"
    "ds_read_b128 v[100:103], v14 offset:16384\n"
    "ds_read_b128 v[148:151], v18\n"
    "ds_read_b128 v[152:155], v19\n"
    "s_barrier\n"
    "ds_read_b64_tr_b16 v[120:121], v10 offset:32768\n"
    "ds_read_b64_tr_b16 v[122:123], v10 offset:33280\n"
    "ds_read_b64_tr_b16 v[124:125], v10 offset:40960\n"
    "ds_read_b64_tr_b16 v[126:127], v10 offset:41472\n"
    "ds_read_b64_tr_b16 v[116:117], v10 offset:24576\n"
    "ds_read_b64_tr_b16 v[118:119], v10 offset:25088\n"
    "s_waitcnt lgkmcnt(7)\n"
    "v_mfma_f32_16x16x32_bf16 v[84:87], v[148:151], v[148:151], v[84:87]\n"
    "ds_read_b128 v[104:107], v15\n"
    "v_mfma_f32_16x16x32_bf16 v[88:91], v[32:35], v[148:151], v[88:91]\n"
    "ds_read_b128 v[108:111], v15 offset:8192\n"
    "s_waitcnt lgkmcnt(8)\n"
    "v_mfma_f32_16x16x32_bf16 v[84:87], v[152:155], v[152:155], v[84:87]\n"
    "ds_read_b128 v[112:115], v15 offset:16384\n"
    "v_mfma_f32_16x16x32_bf16 v[88:91], v[32:35], v[152:155], v[88:91]\n"
    "ds_read_b64_tr_b16 v[132:133], v10 offset:36864\n"
    "v_cndmask_b32 v92, v92, v32, s[74:75]\n"
    "v_cndmask_b32 v93, v93, v32, s[74:75]\n"
    "v_cndmask_b32 v94, v94, v32, s[74:75]\n"
    "v_cndmask_b32 v95, v95, v32, s[74:75]\n"
    "s_waitcnt lgkmcnt(8)\n"
    "v_mfma_f32_16x16x32_bf16 v[44:47], v[120:123], v[96:99], v[44:47]\n"
    "ds_read_b64_tr_b16 v[134:135], v10 offset:37376\n"
    "s_waitcnt lgkmcnt(7)\n"
    "v_mfma_f32_16x16x32_bf16 v[56:59], v[96:99], v[124:127], v[56:59]\n"
    "ds_read_b64_tr_b16 v[136:137], v10 offset:45056\n"
    "s_waitcnt lgkmcnt(6)\n"
    "v_mfma_f32_16x16x32_bf16 v[64:67], v[116:119], v[96:99], v[64:67]\n"
    "ds_read_b64_tr_b16 v[138:139], v10 offset:45568\n"
    "v_mfma_f32_16x16x32_bf16 v[48:51], v[120:123], v[100:103], v[48:51]\n"
    "ds_read_b64_tr_b16 v[128:129], v10 offset:28672\n"
    "v_mfma_f32_16x16x32_bf16 v[60:63], v[100:103], v[124:127], v[60:63]\n"
    "ds_read_b64_tr_b16 v[130:131], v10 offset:29184\n"
    "v_mfma_f32_16x16x32_bf16 v[68:71], v[116:119], v[100:103], v[68:71]\n"
    "v_mfma_f32_16x16x32_bf16 v[40:43], v[120:123], v[92:95], v[40:43]\n"
    "v_mfma_f32_16x16x32_bf16 v[52:55], v[92:95], v[124:127], v[52:55]\n"
    "v_mfma_f32_16x16x32_bf16 v[72:75], v[116:119], v[32:35], v[72:75]\n"
    "s_waitcnt lgkmcnt(8)\n"
    "v_cndmask_b32 v104, v104, v32, s[74:75]\n"
    "v_cndmask_b32 v105, v105, v32, s[74:75]\n"
    "v_cndmask_b32 v106, v106, v32, s[74:75]\n"
    "v_cndmask_b32 v107, v107, v32, s[74:75]\n"
    "s_waitcnt lgkmcnt(4)\n"
    "v_mfma_f32_16x16x32_bf16 v[44:47], v[132:135], v[108:111], v[44:47]\n"
    "s_waitcnt lgkmcnt(2)\n"
    "v_mfma_f32_16x16x32_bf16 v[56:59], v[108:111], v[136:139], v[56:59]\n"
    "s_waitcnt lgkmcnt(0)\n"
    "v_mfma_f32_16x16x32_bf16 v[64:67], v[128:131], v[108:111], v[64:67]\n"
    "v_mfma_f32_16x16x32_bf16 v[48:51], v[132:135], v[112:115], v[48:51]\n"
    "v_mfma_f32_16x16x32_bf16 v[60:63], v[112:115], v[136:139], v[60:63]\n"
    "v_mfma_f32_16x16x32_bf16 v[68:71], v[128:131], v[112:115], v[68:71]\n"
    "v_mfma_f32_16x16x32_bf16 v[40:43], v[132:135], v[104:107], v[40:43]\n"
    "v_mfma_f32_16x16x32_bf16 v[52:55], v[104:107], v[136:139], v[52:55]\n"
    "v_mfma_f32_16x16x32_bf16 v[72:75], v[128:131], v[32:35], v[72:75]\n"
    "ds_read_b128 v[92:95], v12 offset:256\n"
    "ds_read_b128 v[96:99], v12 offset:8448\n"
    "ds_read_b128 v[100:103], v12 offset:16640\n"
    "ds_read_b128 v[140:143], v16 offset:256\n"
    "ds_read_b128 v[144:147], v17 offset:256\n"
    "s_barrier\n"
    "ds_read_b64_tr_b16 v[120:121], v11 offset:8192\n"
    "ds_read_b64_tr_b16 v[122:123], v11 offset:8704\n"
    "ds_read_b64_tr_b16 v[124:125], v11 offset:16384\n"
    "ds_read_b64_tr_b16 v[126:127], v11 offset:16896\n"
    "ds_read_b64_tr_b16 v[116:117], v11\n"
    "ds_read_b64_tr_b16 v[118:119], v11 offset:512\n"
    "s_waitcnt lgkmcnt(7)\n"
    "v_mfma_f32_16x16x32_bf16 v[84:87], v[140:143], v[140:143], v[84:87]\n"
    "buffer_store_dwordx4 v[36:39], v1, s[36:39], s68 offen sc1\n"
    "ds_read_b128 v[104:107], v13 offset:256\n"
    "v_mfma_f32_16x16x32_bf16 v[88:91], v[32:35], v[140:143], v[88:91]\n"
    "ds_read_b128 v[108:111], v13 offset:8448\n"
    "s_waitcnt lgkmcnt(8)\n"
    "v_mfma_f32_16x16x32_bf16 v[84:87], v[144:147], v[144:147], v[84:87]\n"
    "buffer_store_dwordx4 v[36:39], v1, s[36:39], s68 offen offset:1024 sc1\n"
    "ds_read_b128 v[112:115], v13 offset:16640\n"
    "v_mfma_f32_16x16x32_bf16 v[88:91], v[32:35], v[144:147], v[88:91]\n"
    "ds_read_b64_tr_b16 v[132:133], v11 offset:12288\n"
    "v_cndmask_b32 v92, v92, v32, s[74:75]\n"
    "v_cndmask_b32 v93, v93, v32, s[74:75]\n"
    "v_cndmask_b32 v94, v94, v32, s[74:75]\n"
    "v_cndmask_b32 v95, v95, v32, s[74:75]\n"
    "s_waitcnt lgkmcnt(8)\n"
    "v_mfma_f32_16x16x32_bf16 v[44:47], v[120:123], v[96:99], v[44:47]\n"
    "buffer_store_dwordx4 v[36:39], v1, s[36:39], s68 offen offset:2048 sc1\n"
    "ds_read_b64_tr_b16 v[134:135], v11 offset:12800\n"
    "s_waitcnt lgkmcnt(7)\n"
    "v_mfma_f32_16x16x32_bf16 v[56:59], v[96:99], v[124:127], v[56:59]\n"
    "ds_read_b64_tr_b16 v[136:137], v11 offset:20480\n"
    "s_waitcnt lgkmcnt(6)\n"
    "v_mfma_f32_16x16x32_bf16 v[64:67], v[116:119], v[96:99], v[64:67]\n"
    "buffer_store_dwordx4 v[36:39], v1, s[36:39], s68 offen offset:3072 sc1\n"
    "ds_read_b64_tr_b16 v[138:139], v11 offset:20992\n"
    "v_mfma_f32_16x16x32_bf16 v[48:51], v[120:123], v[100:103], v[48:51]\n"
    "ds_read_b64_tr_b16 v[128:129], v11 offset:4096\n"
    "v_mfma_f32_16x16x32_bf16 v[60:63], v[100:103], v[124:127], v[60:63]\n"
    "buffer_store_dwordx4 v[36:39], v1, s[36:39], s69 offen sc1\n"
    "ds_read_b64_tr_b16 v[130:131], v11 offset:4608\n"
    "v_mfma_f32_16x16x32_bf16 v[68:71], v[116:119], v[100:103], v[68:71]\n"
    "v_mfma_f32_16x16x32_bf16 v[40:43], v[120:123], v[92:95], v[40:43]\n"
    "buffer_store_dwordx4 v[36:39], v1, s[36:39], s69 offen offset:1024 sc1\n"
    "v_mfma_f32_16x16x32_bf16 v[52:55], v[92:95], v[124:127], v[52:55]\n"
    "v_mfma_f32_16x16x32_bf16 v[72:75], v[116:119], v[32:35], v[72:75]\n"
    "s_waitcnt lgkmcnt(8)\n"
    "v_cndmask_b32 v104, v104, v32, s[74:75]\n"
    "v_cndmask_b32 v105, v105, v32, s[74:75]\n"
    "v_cndmask_b32 v106, v106, v32, s[74:75]\n"
    "v_cndmask_b32 v107, v107, v32, s[74:75]\n"
    "s_waitcnt lgkmcnt(4)\n"
    "v_mfma_f32_16x16x32_bf16 v[44:47], v[132:135], v[108:111], v[44:47]\n"
    "s_waitcnt lgkmcnt(2)\n"
    "v_mfma_f32_16x16x32_bf16 v[56:59], v[108:111], v[136:139], v[56:59]\n"
    "s_waitcnt lgkmcnt(0)\n"
    "v_mfma_f32_16x16x32_bf16 v[64:67], v[128:131], v[108:111], v[64:67]\n"
    "v_mfma_f32_16x16x32_bf16 v[48:51], v[132:135], v[112:115], v[48:51]\n"
    "v_mfma_f32_16x16x32_bf16 v[60:63], v[112:115], v[136:139], v[60:63]\n"
    "v_mfma_f32_16x16x32_bf16 v[68:71], v[128:131], v[112:115], v[68:71]\n"
    "v_mfma_f32_16x16x32_bf16 v[40:43], v[132:135], v[104:107], v[40:43]\n"
    "v_mfma_f32_16x16x32_bf16 v[52:55], v[104:107], v[136:139], v[52:55]\n"
    "v_mfma_f32_16x16x32_bf16 v[72:75], v[128:131], v[32:35], v[72:75]\n"
    "ds_read_b128 v[92:95], v14 offset:256\n"
    "ds_read_b128 v[96:99], v14 offset:8448\n"
    "ds_read_b128 v[100:103], v14 offset:16640\n"
    "ds_read_b128 v[148:151], v18 offset:256\n"
    "ds_read_b128 v[152:155], v19 offset:256\n"
    "s_barrier\n"
    "ds_read_b64_tr_b16 v[120:121], v10 offset:8192\n"
    "ds_read_b64_tr_b16 v[122:123], v10 offset:8704\n"
    "ds_read_b64_tr_b16 v[124:125], v10 offset:16384\n"
    "ds_read_b64_tr_b16 v[126:127], v10 offset:16896\n"
    "ds_read_b64_tr_b16 v[116:117], v10\n"
    "ds_read_b64_tr_b16 v[118:119], v10 offset:512\n"
    "s_waitcnt lgkmcnt(7)\n"
    "v_mfma_f32_16x16x32_bf16 v[84:87], v[148:151], v[148:151], v[84:87]\n"
    "ds_read_b128 v[104:107], v15 offset:256\n"
    "v_mfma_f32_16x16x32_bf16 v[88:91], v[32:35], v[148:151], v[88:91]\n"
    "ds_read_b128 v[108:111], v15 offset:8448\n"
    "s_waitcnt lgkmcnt(8)\n"
    "v_mfma_f32_16x16x32_bf16 v[84:87], v[152:155], v[152:155], v[84:87]\n"
    "ds_read_b128 v[112:115], v15 offset:16640\n"
    "v_mfma_f32_16x16x32_bf16 v[88:91], v[32:35], v[152:155], v[88:91]\n"
    "ds_read_b64_tr_b16 v[132:133], v10 offset:12288\n"
    "v_cndmask_b32 v92, v92, v32, s[74:75]\n"
    "v_cndmask_b32 v93, v93, v32, s[74:75]\n"
    "v_cndmask_b32 v94, v94, v32, s[74:75]\n"
    "v_cndmask_b32 v95, v95, v32, s[74:75]\n"
    "s_waitcnt lgkmcnt(8)\n"
    "v_mfma_f32_16x16x32_bf16 v[44:47], v[120:123], v[96:99], v[44:47]\n"
    "ds_read_b64_tr_b16 v[134:135], v10 offset:12800\n"
    "s_waitcnt lgkmcnt(7)\n"
    "v_mfma_f32_16x16x32_bf16 v[56:59], v[96:99], v[124:127], v[56:59]\n"
    "ds_read_b64_tr_b16 v[136:137], v10 offset:20480\n"
    "s_waitcnt lgkmcnt(6)\n"
    "v_mfma_f32_16x16x32_bf16 v[64:67], v[116:119], v[96:99], v[64:67]\n"
    "ds_read_b64_tr_b16 v[138:139], v10 offset:20992\n"
    "v_mfma_f32_16x16x32_bf16 v[48:51], v[120:123], v[100:103], v[48:51]\n"
    "ds_read_b64_tr_b16 v[128:129], v10 offset:4096\n"
    "v_mfma_f32_16x16x32_bf16 v[60:63], v[100:103], v[124:127], v[60:63]\n"
    "ds_read_b64_tr_b16 v[130:131], v10 offset:4608\n"
    "v_mfma_f32_16x16x32_bf16 v[68:71], v[116:119], v[100:103], v[68:71]\n"
    "v_mfma_f32_16x16x32_bf16 v[40:43], v[120:123], v[92:95], v[40:43]\n"
    "v_mfma_f32_16x16x32_bf16 v[52:55], v[92:95], v[124:127], v[52:55]\n"
    "v_mfma_f32_16x16x32_bf16 v[72:75], v[116:119], v[32:35], v[72:75]\n"
    "s_waitcnt lgkmcnt(8)\n"
    "v_cndmask_b32 v104, v104, v32, s[74:75]\n"
    "v_cndmask_b32 v105, v105, v32, s[74:75]\n"
    "v_cndmask_b32 v106, v106, v32, s[74:75]\n"
    "v_cndmask_b32 v107, v107, v32, s[74:75]\n"
    "s_waitcnt lgkmcnt(4)\n"
    "v_mfma_f32_16x16x32_bf16 v[44:47], v[132:135], v[108:111], v[44:47]\n"
    "s_waitcnt lgkmcnt(2)\n"
    "v_mfma_f32_16x16x32_bf16 v[56:59], v[108:111], v[136:139], v[56:59]\n"
    "s_waitcnt lgkmcnt(0)\n"
    "v_mfma_f32_16x16x32_bf16 v[64:67], v[128:131], v[108:111], v[64:67]\n"
    "v_mfma_f32_16x16x32_bf16 v[48:51], v[132:135], v[112:115], v[48:51]\n"
    "v_mfma_f32_16x16x32_bf16 v[60:63], v[112:115], v[136:139], v[60:63]\n"
    "v_mfma_f32_16x16x32_bf16 v[68:71], v[128:131], v[112:115], v[68:71]\n"
    "v_mfma_f32_16x16x32_bf16 v[40:43], v[132:135], v[104:107], v[40:43]\n"
    "v_mfma_f32_16x16x32_bf16 v[52:55], v[104:107], v[136:139], v[52:55]\n"
    "v_mfma_f32_16x16x32_bf16 v[72:75], v[128:131], v[32:35], v[72:75]\n"
    "s_waitcnt vmcnt(0)\n"
    "s_barrier\n"
    "ds_read_b64_tr_b16 v[120:121], v10 offset:32768\n"
    "ds_read_b64_tr_b16 v[122:123], v10 offset:33280\n"
    "ds_read_b64_tr_b16 v[124:125], v10 offset:40960\n"
    "ds_read_b64_tr_b16 v[126:127], v10 offset:41472\n"
    "ds_read_b64_tr_b16 v[116:117], v10 offset:24576\n"
    "ds_read_b64_tr_b16 v[118:119], v10 offset:25088\n"
    "ds_read_b128 v[92:95], v12 offset:40960\n"
    "ds_read_b128 v[96:99], v12 offset:49152\n"
    "ds_read_b128 v[100:103], v12 offset:57344\n"
    "ds_read_b128 v[140:143], v16 offset:40960\n"
    "ds_read_b128 v[144:147], v17 offset:40960\n"
    "ds_read_b128 v[148:151], v18 offset:40960\n"
    "ds_read_b128 v[152:155], v19 offset:40960\n"
    "s_waitcnt lgkmcnt(3)\n"
    "v_mfma_f32_16x16x32_bf16 v[84:87], v[140:143], v[140:143], v[84:87]\n"
    "s_cmp_eq_u32 s53, 0\n"
    "s_cbranch_scc0 .Lflag_done\n"
    "v_mov_b32 v20, s56\n"
    "v_mov_b32 v21, 0\n"
    "s_lshl_b32 s60, s48, 2\n"
    "s_add_u32 s60, s60, 0xe88400\n"
    "s_mov_b64 exec, 1\n"
    "buffer_store_dword v20, v21, s[36:39], s60 offen sc1\n"
    "s_mov_b64 exec, -1\n"
    ".Lflag_done:\n"
    "ds_read_b128 v[104:107], v13 offset:40960\n"
    "v_mfma_f32_16x16x32_bf16 v[88:91], v[32:35], v[140:143], v[88:91]\n"
    "ds_read_b128 v[108:111], v13 offset:49152\n"
    "s_waitcnt lgkmcnt(4)\n"
    "v_mfma_f32_16x16x32_bf16 v[84:87], v[144:147], v[144:147], v[84:87]\n"
    "ds_read_b128 v[112:115], v13 offset:57344\n"
    "v_mfma_f32_16x16x32_bf16 v[88:91], v[32:35], v[144:147], v[88:91]\n"
    "ds_read_b64_tr_b16 v[132:133], v10 offset:36864\n"
    "s_waitcnt lgkmcnt(5)\n"
    "v_mfma_f32_16x16x32_bf16 v[84:87], v[148:151], v[148:151], v[84:87]\n"
    "ds_read_b64_tr_b16 v[134:135], v10 offset:37376\n"
    "v_mfma_f32_16x16x32_bf16 v[88:91], v[32:35], v[148:151], v[88:91]\n"
    "ds_read_b64_tr_b16 v[136:137], v10 offset:45056\n"
    "s_waitcnt lgkmcnt(6)\n"
    "v_mfma_f32_16x16x32_bf16 v[84:87], v[152:155], v[152:155], v[84:87]\n"
    "ds_read_b64_tr_b16 v[138:139], v10 offset:45568\n"
    "v_mfma_f32_16x16x32_bf16 v[88:91], v[32:35], v[152:155], v[88:91]\n"
    "ds_read_b64_tr_b16 v[128:129], v10 offset:28672\n"
    "v_cndmask_b32 v92, v92, v32, s[74:75]\n"
    "v_cndmask_b32 v93, v93, v32, s[74:75]\n"
    "v_cndmask_b32 v94, v94, v32, s[74:75]\n"
    "v_cndmask_b32 v95, v95, v32, s[74:75]\n"
    "v_mfma_f32_16x16x32_bf16 v[44:47], v[120:123], v[96:99], v[44:47]\n"
    "ds_read_b64_tr_b16 v[130:131], v10 offset:29184\n"
    "v_mfma_f32_16x16x32_bf16 v[56:59], v[96:99], v[124:127], v[56:59]\n"
    "v_mfma_f32_16x16x32_bf16 v[64:67], v[116:119], v[96:99], v[64:67]\n"
    "v_mfma_f32_16x16x32_bf16 v[48:51], v[120:123], v[100:103], v[48:51]\n"
    "v_mfma_f32_16x16x32_bf16 v[60:63], v[100:103], v[124:127], v[60:63]\n"
    "v_mfma_f32_16x16x32_bf16 v[68:71], v[116:119], v[100:103], v[68:71]\n"
    "v_mfma_f32_16x16x32_bf16 v[40:43], v[120:123], v[92:95], v[40:43]\n"
    "v_mfma_f32_16x16x32_bf16 v[52:55], v[92:95], v[124:127], v[52:55]\n"
    "v_mfma_f32_16x16x32_bf16 v[72:75], v[116:119], v[32:35], v[72:75]\n"
    "s_waitcnt lgkmcnt(8)\n"
    "v_cndmask_b32 v104, v104, v32, s[74:75]\n"
    "v_cndmask_b32 v105, v105, v32, s[74:75]\n"
    "v_cndmask_b32 v106, v106, v32, s[74:75]\n"
    "v_cndmask_b32 v107, v107, v32, s[74:75]\n"
    "s_waitcnt lgkmcnt(4)\n"
    "v_mfma_f32_16x16x32_bf16 v[44:47], v[132:135], v[108:111], v[44:47]\n"
    "s_waitcnt lgkmcnt(2)\n"
    "v_mfma_f32_16x16x32_bf16 v[56:59], v[108:111], v[136:139], v[56:59]\n"
    "s_waitcnt lgkmcnt(0)\n"
    "v_mfma_f32_16x16x32_bf16 v[64:67], v[128:131], v[108:111], v[64:67]\n"
    "v_mfma_f32_16x16x32_bf16 v[48:51], v[132:135], v[112:115], v[48:51]\n"
    "v_mfma_f32_16x16x32_bf16 v[60:63], v[112:115], v[136:139], v[60:63]\n"
    "v_mfma_f32_16x16x32_bf16 v[68:71], v[128:131], v[112:115], v[68:71]\n"
    "v_mfma_f32_16x16x32_bf16 v[40:43], v[132:135], v[104:107], v[40:43]\n"
    "v_mfma_f32_16x16x32_bf16 v[52:55], v[104:107], v[136:139], v[52:55]\n"
    "v_mfma_f32_16x16x32_bf16 v[72:75], v[128:131], v[32:35], v[72:75]\n"
    "ds_read_b128 v[92:95], v14 offset:40960\n"
    "ds_read_b128 v[96:99], v14 offset:49152\n"
    "ds_read_b128 v[100:103], v14 offset:57344\n"
    "ds_read_b128 v[140:143], v16 offset:41216\n"
    "ds_read_b128 v[144:147], v17 offset:41216\n"
    "ds_read_b128 v[148:151], v18 offset:41216\n"
    "ds_read_b128 v[152:155], v19 offset:41216\n"
    "s_barrier\n"
    "ds_read_b64_tr_b16 v[120:121], v11 offset:8192\n"
    "ds_read_b64_tr_b16 v[122:123], v11 offset:8704\n"
    "ds_read_b64_tr_b16 v[124:125], v11 offset:16384\n"
    "ds_read_b64_tr_b16 v[126:127], v11 offset:16896\n"
    "ds_read_b64_tr_b16 v[116:117], v11\n"
    "ds_read_b64_tr_b16 v[118:119], v11 offset:512\n"
    "s_waitcnt lgkmcnt(9)\n"
    "v_mfma_f32_16x16x32_bf16 v[84:87], v[140:143], v[140:143], v[84:87]\n"
    "ds_read_b128 v[104:107], v15 offset:40960\n"
    "v_mfma_f32_16x16x32_bf16 v[88:91], v[32:35], v[140:143], v[88:91]\n"
    "ds_read_b128 v[108:111], v15 offset:49152\n"
    "s_waitcnt lgkmcnt(10)\n"
    "v_mfma_f32_16x16x32_bf16 v[84:87], v[144:147], v[144:147], v[84:87]\n"
    "ds_read_b128 v[112:115], v15 offset:57344\n"
    "v_mfma_f32_16x16x32_bf16 v[88:91], v[32:35], v[144:147], v[88:91]\n"
    "ds_read_b64_tr_b16 v[132:133], v11 offset:12288\n"
    "s_waitcnt lgkmcnt(11)\n"
    "v_mfma_f32_16x16x32_bf16 v[84:87], v[148:151], v[148:151], v[84:87]\n"
    "ds_read_b64_tr_b16 v[134:135], v11 offset:12800\n"
    "v_mfma_f32_16x16x32_bf16 v[88:91], v[32:35], v[148:151], v[88:91]\n"
    "ds_read_b64_tr_b16 v[136:137], v11 offset:20480\n"
    "s_waitcnt lgkmcnt(12)\n"
    "v_mfma_f32_16x16x32_bf16 v[84:87], v[152:155], v[152:155], v[84:87]\n"
    "ds_read_b64_tr_b16 v[138:139], v11 offset:20992\n"
    "v_mfma_f32_16x16x32_bf16 v[88:91], v[32:35], v[152:155], v[88:91]\n"
    "ds_read_b64_tr_b16 v[128:129], v11 offset:4096\n"
    "v_cndmask_b32 v92, v92, v32, s[74:75]\n"
    "v_cndmask_b32 v93, v93, v32, s[74:75]\n"
    "v_cndmask_b32 v94, v94, v32, s[74:75]\n"
    "v_cndmask_b32 v95, v95, v32, s[74:75]\n"
    "s_waitcnt lgkmcnt(12)\n"
    "v_mfma_f32_16x16x32_bf16 v[44:47], v[120:123], v[96:99], v[44:47]\n"
    "ds_read_b64_tr_b16 v[130:131], v11 offset:4608\n"
    "s_waitcnt lgkmcnt(11)\n"
    "v_mfma_f32_16x16x32_bf16 v[56:59], v[96:99], v[124:127], v[56:59]\n"
    "s_waitcnt lgkmcnt(9)\n"
    "v_mfma_f32_16x16x32_bf16 v[64:67], v[116:119], v[96:99], v[64:67]\n"
    "v_mfma_f32_16x16x32_bf16 v[48:51], v[120:123], v[100:103], v[48:51]\n"
    "v_mfma_f32_16x16x32_bf16 v[60:63], v[100:103], v[124:127], v[60:63]\n"
    "v_mfma_f32_16x16x32_bf16 v[68:71], v[116:119], v[100:103], v[68:71]\n"
    "v_mfma_f32_16x16x32_bf16 v[40:43], v[120:123], v[92:95], v[40:43]\n"
    "v_mfma_f32_16x16x32_bf16 v[52:55], v[92:95], v[124:127], v[52:55]\n"
    "v_mfma_f32_16x16x32_bf16 v[72:75], v[116:119], v[32:35], v[72:75]\n"
    "s_waitcnt lgkmcnt(8)\n"
    "v_cndmask_b32 v104, v104, v32, s[74:75]\n"
    "v_cndmask_b32 v105, v105, v32, s[74:75]\n"
    "v_cndmask_b32 v106, v106, v32, s[74:75]\n"
    "v_cndmask_b32 v107, v107, v32, s[74:75]\n"
    "s_waitcnt lgkmcnt(4)\n"
    "v_mfma_f32_16x16x32_bf16 v[44:47], v[132:135], v[108:111], v[44:47]\n"
    "s_waitcnt lgkmcnt(2)\n"
    "v_mfma_f32_16x16x32_bf16 v[56:59], v[108:111], v[136:139], v[56:59]\n"
    "s_waitcnt lgkmcnt(0)\n"
    "v_mfma_f32_16x16x32_bf16 v[64:67], v[128:131], v[108:111], v[64:67]\n"
    "v_mfma_f32_16x16x32_bf16 v[48:51], v[132:135], v[112:115], v[48:51]\n"
    "v_mfma_f32_16x16x32_bf16 v[60:63], v[112:115], v[136:139], v[60:63]\n"
    "v_mfma_f32_16x16x32_bf16 v[68:71], v[128:131], v[112:115], v[68:71]\n"
    "v_mfma_f32_16x16x32_bf16 v[40:43], v[132:135], v[104:107], v[40:43]\n"
    "v_mfma_f32_16x16x32_bf16 v[52:55], v[104:107], v[136:139], v[52:55]\n"
    "v_mfma_f32_16x16x32_bf16 v[72:75], v[128:131], v[32:35], v[72:75]\n"
    "ds_read_b128 v[92:95], v12 offset:41216\n"
    "ds_read_b128 v[96:99], v12 offset:49408\n"
    "ds_read_b128 v[100:103], v12 offset:57600\n"
    "s_barrier\n"
    "ds_read_b64_tr_b16 v[120:121], v10 offset:8192\n"
    "ds_read_b64_tr_b16 v[122:123], v10 offset:8704\n"
    "ds_read_b64_tr_b16 v[124:125], v10 offset:16384\n"
    "ds_read_b64_tr_b16 v[126:127], v10 offset:16896\n"
    "ds_read_b64_tr_b16 v[116:117], v10\n"
    "ds_read_b64_tr_b16 v[118:119], v10 offset:512\n"
    "s_waitcnt lgkmcnt(8)\n"
    "v_cndmask_b32 v92, v92, v32, s[74:75]\n"
    "v_cndmask_b32 v93, v93, v32, s[74:75]\n"
    "v_cndmask_b32 v94, v94, v32, s[74:75]\n"
    "v_cndmask_b32 v95, v95, v32, s[74:75]\n"
    "s_waitcnt lgkmcnt(4)\n"
    "v_mfma_f32_16x16x32_bf16 v[44:47], v[120:123], v[96:99], v[44:47]\n"
    "s_mov_b32 s60, 0xe88400\n"
    "buffer_load_dwordx4 v[156:159], v1, s[36:39], s60 offen sc1\n"
    "ds_read_b128 v[104:107], v13 offset:41216\n"
    "s_waitcnt lgkmcnt(3)\n"
    "v_mfma_f32_16x16x32_bf16 v[56:59], v[96:99], v[124:127], v[56:59]\n"
    "v_and_b32 v22, 3, v3\n"
    "v_cmp_eq_u32 vcc, 1, v22\n"
    "s_nop 1\n"
    "v_cndmask_b32 v23, v84, v85, vcc\n"
    "ds_read_b128 v[108:111], v13 offset:49408\n"
    "s_waitcnt lgkmcnt(2)\n"
    "v_mfma_f32_16x16x32_bf16 v[64:67], v[116:119], v[96:99], v[64:67]\n"
    "v_cmp_eq_u32 vcc, 2, v22\n"
    "s_nop 1\n"
    "v_cndmask_b32 v23, v23, v86, vcc\n"
    "v_cmp_eq_u32 vcc, 3, v22\n"
    "s_nop 1\n"
    "v_cndmask_b32 v23, v23, v87, vcc\n"
    "ds_read_b128 v[112:115], v13 offset:57600\n"
    "v_mfma_f32_16x16x32_bf16 v[48:51], v[120:123], v[100:103], v[48:51]\n"
    "v_mul_f32 v26, 0x3b000000, v88\n"
    "v_mul_f32 v23, 0x3b000000, v23\n"
    "ds_read_b64_tr_b16 v[132:133], v10 offset:12288\n"
    "v_mfma_f32_16x16x32_bf16 v[60:63], v[100:103], v[124:127], v[60:63]\n"
    "v_fma_f32 v23, -v26, v26, v23\n"
    "v_max_f32 v23, 0, v23\n"
    "ds_read_b64_tr_b16 v[134:135], v10 offset:12800\n"
    "v_mfma_f32_16x16x32_bf16 v[68:71], v[116:119], v[100:103], v[68:71]\n"
    "v_add_f32 v23, 0x3727c5ac, v23\n"
    "v_rsq_f32 v27, v23\n"
    "ds_read_b64_tr_b16 v[136:137], v10 offset:20480\n"
    "v_mfma_f32_16x16x32_bf16 v[40:43], v[120:123], v[92:95], v[40:43]\n"
    "s_add_u32 s60, s53, 1\n"
    "s_mul_i32 s60, s60, s57\n"
    "ds_read_b64_tr_b16 v[138:139], v10 offset:20992\n"
    "v_mfma_f32_16x16x32_bf16 v[52:55], v[92:95], v[124:127], v[52:55]\n"
    "s_lshl_b32 s60, s60, 4\n"
    "v_add_u32 v20, s60, v3\n"
    "ds_read_b64_tr_b16 v[128:129], v10 offset:4096\n"
    "v_mfma_f32_16x16x32_bf16 v[72:75], v[116:119], v[32:35], v[72:75]\n"
    "v_lshrrev_b32 v22, 2, v3\n"
    "v_cmp_eq_u32 vcc, v22, v2\n"
    "s_nop 1\n"
    "v_cndmask_b32 v20, v32, v20, vcc\n"
    "ds_read_b64_tr_b16 v[130:131], v10 offset:4608\n"
    "s_waitcnt lgkmcnt(8)\n"
    "v_cndmask_b32 v104, v104, v32, s[74:75]\n"
    "v_cndmask_b32 v105, v105, v32, s[74:75]\n"
    "v_cndmask_b32 v106, v106, v32, s[74:75]\n"
    "v_cndmask_b32 v107, v107, v32, s[74:75]\n"
    "s_waitcnt lgkmcnt(4)\n"
    "v_mfma_f32_16x16x32_bf16 v[44:47], v[132:135], v[108:111], v[44:47]\n"
    "v_min_u32 v20, 0xff, v20\n"
    "v_lshlrev_b32 v21, 2, v20\n"
    "s_waitcnt lgkmcnt(2)\n"
    "v_mfma_f32_16x16x32_bf16 v[56:59], v[108:111], v[136:139], v[56:59]\n"
    "v_lshlrev_b32 v20, 3, v20\n"
    "v_add_u32 v20, 0x26000, v20\n"
    "s_waitcnt lgkmcnt(0)\n"
    "v_mfma_f32_16x16x32_bf16 v[64:67], v[128:131], v[108:111], v[64:67]\n"
    "v_add_u32 v21, 0x26800, v21\n"
    "ds_write_b64 v20, v[26:27]\n"
    "v_mfma_f32_16x16x32_bf16 v[48:51], v[132:135], v[112:115], v[48:51]\n"
    "ds_write_b32 v21, v26\n"
    "ds_write_b32 v21, v27 offset:1024\n"
    "v_mfma_f32_16x16x32_bf16 v[60:63], v[112:115], v[136:139], v[60:63]\n"
    "v_mfma_f32_16x16x32_bf16 v[68:71], v[128:131], v[112:115], v[68:71]\n"
    "v_mfma_f32_16x16x32_bf16 v[40:43], v[132:135], v[104:107], v[40:43]\n"
    "v_mfma_f32_16x16x32_bf16 v[52:55], v[104:107], v[136:139], v[52:55]\n"
    "v_mfma_f32_16x16x32_bf16 v[72:75], v[128:131], v[32:35], v[72:75]\n"
    "s_waitcnt vmcnt(0)\n"
    "v_xor_b32 v156, s56, v156\n"
    "v_xor_b32 v157, s56, v157\n"
    "v_xor_b32 v158, s56, v158\n"
    "v_xor_b32 v159, s56, v159\n"
    "v_or3_b32 v156, v156, v157, v158\n"
    "v_or_b32 v156, v156, v159\n"
    "v_cmp_ne_u32 vcc, 0, v156\n"
    "s_nop 4\n"
    "s_cbranch_vccnz .Lpoll_slow\n"
    ".Lpoll_ok:\n"
    ".Lp1_join:\n"
    "ds_read_b128 v[92:95], v14 offset:41216\n"
    "ds_read_b128 v[96:99], v14 offset:49408\n"
    "ds_read_b128 v[100:103], v14 offset:57600\n"
    "s_waitcnt lgkmcnt(0)\n"
    "s_barrier\n"
    "ds_read_b64_tr_b16 v[120:121], v10 offset:32768\n"
    "ds_read_b64_tr_b16 v[122:123], v10 offset:33280\n"
    "ds_read_b64_tr_b16 v[124:125], v10 offset:40960\n"
    "ds_read_b64_tr_b16 v[126:127], v10 offset:41472\n"
    "ds_read_b64_tr_b16 v[116:117], v10 offset:24576\n"
    "ds_read_b64_tr_b16 v[118:119], v10 offset:25088\n"
    "v_cndmask_b32 v92, v92, v32, s[74:75]\n"
    "v_cndmask_b32 v93, v93, v32, s[74:75]\n"
    "v_cndmask_b32 v94, v94, v32, s[74:75]\n"
    "v_cndmask_b32 v95, v95, v32, s[74:75]\n"
    "s_waitcnt lgkmcnt(4)\n"
    "v_mfma_f32_16x16x32_bf16 v[44:47], v[120:123], v[96:99], v[44:47]\n"
    "s_lshl_b32 s33, s49, 10\n"
    "s_lshl_b32 s35, s51, 16\n"
    "s_add_u32 s35, s35, s33\n"
    "ds_read_b128 v[104:107], v15 offset:41216\n"
    "s_waitcnt lgkmcnt(3)\n"
    "v_mfma_f32_16x16x32_bf16 v[56:59], v[96:99], v[124:127], v[56:59]\n"
    "s_add_u32 s64, s35, 0\n"
    "s_add_u32 m0, s33, 0x14000\n"
    "s_nop 0\n"
    "buffer_load_dwordx4 v1, s[36:39], s64 offen sc1 lds\n"
    "ds_read_b128 v[108:111], v15 offset:49408\n"
    "s_waitcnt lgkmcnt(2)\n"
    "v_mfma_f32_16x16x32_bf16 v[64:67], v[116:119], v[96:99], v[64:67]\n"
    "s_add_u32 s64, s35, 0x2000\n"
    "s_add_u32 m0, s33, 0x16000\n"
    "s_nop 0\n"
    "buffer_load_dwordx4 v1, s[36:39], s64 offen sc1 lds\n"
    "ds_read_b128 v[112:115], v15 offset:57600\n"
    "v_mfma_f32_16x16x32_bf16 v[48:51], v[120:123], v[100:103], v[48:51]\n"
    "s_add_u32 s64, s35, 0x4000\n"
    "s_add_u32 m0, s33, 0x18000\n"
    "s_nop 0\n"
    "buffer_load_dwordx4 v1, s[36:39], s64 offen sc1 lds\n"
    "ds_read_b64_tr_b16 v[132:133], v10 offset:36864\n"
    "v_mfma_f32_16x16x32_bf16 v[60:63], v[100:103], v[124:127], v[60:63]\n"
    "s_add_u32 s64, s35, 0xc000\n"
    "s_add_u32 m0, s33, 0x20000\n"
    "s_nop 0\n"
    "buffer_load_dwordx4 v1, s[36:39], s64 offen sc1 lds\n"
    "ds_read_b64_tr_b16 v[134:135], v10 offset:37376\n"
    "v_mfma_f32_16x16x32_bf16 v[68:71], v[116:119], v[100:103], v[68:71]\n"
    "s_add_u32 s64, s35, 0xe000\n"
    "s_add_u32 m0, s33, 0x22000\n"
    "s_nop 0\n"
    "buffer_load_dwordx4 v1, s[36:39], s64 offen sc1 lds\n"
    "ds_read_b64_tr_b16 v[136:137], v10 offset:45056\n"
    "v_mfma_f32_16x16x32_bf16 v[40:43], v[120:123], v[92:95], v[40:43]\n"
    "s_lshl_b32 s66, s54, 14\n"
    "s_lshl_b32 s60, s57, 13\n"
    "s_add_u32 s66, s66, s60\n"
    "ds_read_b64_tr_b16 v[138:139], v10 offset:45568\n"
    "v_mfma_f32_16x16x32_bf16 v[52:55], v[92:95], v[124:127], v[52:55]\n"
    "s_add_u32 s66, s66, 0x80000\n"
    "s_add_u32 s67, s66, 0x4000\n"
    "s_add_u32 s68, s66, 0x8000\n"
    "ds_read_b64_tr_b16 v[128:129], v10 offset:28672\n"
    "v_mfma_f32_16x16x32_bf16 v[72:75], v[116:119], v[32:35], v[72:75]\n"
    "s_cmp_eq_u32 s55, 3\n"
    "s_cselect_b32 s68, s68, 0x4000000\n"
    "s_lshl_b32 s60, s51, 1\n"
    "s_add_u32 s60, s60, s57\n"
    "ds_read_b64_tr_b16 v[130:131], v10 offset:29184\n"
    "s_waitcnt lgkmcnt(8)\n"
    "v_cndmask_b32 v104, v104, v32, s[74:75]\n"
    "v_cndmask_b32 v105, v105, v32, s[74:75]\n"
    "v_cndmask_b32 v106, v106, v32, s[74:75]\n"
    "v_cndmask_b32 v107, v107, v32, s[74:75]\n"
    "s_waitcnt lgkmcnt(4)\n"
    "v_mfma_f32_16x16x32_bf16 v[44:47], v[132:135], v[108:111], v[44:47]\n"
    "s_lshr_b32 s61, s60, 3\n"
    "s_mul_i32 s61, s61, 0xa000\n"
    "s_bfe_u32 s62, s60, 0x10002\n"
    "s_waitcnt lgkmcnt(2)\n"
    "v_mfma_f32_16x16x32_bf16 v[56:59], v[108:111], v[136:139], v[56:59]\n"
    "s_lshl_b32 s62, s62, 8\n"
    "s_add_u32 s61, s61, s62\n"
    "s_add_u32 s62, s53, 1\n"
    "s_waitcnt lgkmcnt(0)\n"
    "v_mfma_f32_16x16x32_bf16 v[64:67], v[128:131], v[108:111], v[64:67]\n"
    "s_lshl_b32 s62, s62, 13\n"
    "s_add_u32 s61, s61, s62\n"
    "s_and_b32 s60, s60, 3\n"
    "v_mfma_f32_16x16x32_bf16 v[48:51], v[132:135], v[112:115], v[48:51]\n"
    "s_lshl_b32 s60, s60, 6\n"
    "v_xor_b32 v20, v2, v3\n"
    "v_lshlrev_b32 v20, 4, v20\n"
    "v_mfma_f32_16x16x32_bf16 v[60:63], v[112:115], v[136:139], v[60:63]\n"
    "v_lshl_add_u32 v20, v3, 9, v20\n"
    "v_xor_b32 v20, s60, v20\n"
    "v_add_u32 v20, s61, v20\n"
    "v_mfma_f32_16x16x32_bf16 v[68:71], v[128:131], v[112:115], v[68:71]\n"
    "ds_read_b128 v[4:7], v20\n"
    "v_mfma_f32_16x16x32_bf16 v[40:43], v[132:135], v[104:107], v[40:43]\n"
    "v_mfma_f32_16x16x32_bf16 v[52:55], v[104:107], v[136:139], v[52:55]\n"
    "v_mfma_f32_16x16x32_bf16 v[72:75], v[128:131], v[32:35], v[72:75]\n"
    "s_lshl_b32 s60, s57, 5\n"
    "v_add_u32 v20, s60, v3\n"
    "v_lshlrev_b32 v20, 3, v20\n"
    "v_add_u32 v20, 0x26000, v20\n"
    "v_lshl_add_u32 v21, v2, 2, s60\n"
    "v_lshlrev_b32 v21, 2, v21\n"
    "v_add_u32 v21, 0x26800, v21\n"
    "ds_read_b64 v[92:93], v20\n"
    "ds_read_b64 v[94:95], v20 offset:128\n"
    "ds_read_b64 v[96:97], v20 offset:256\n"
    "ds_read_b128 v[100:103], v21\n"
    "ds_read_b128 v[112:115], v21 offset:1024\n"
    "ds_read_b128 v[104:107], v21 offset:64\n"
    "ds_read_b128 v[116:119], v21 offset:1088\n"
    "ds_read_b128 v[108:111], v21 offset:128\n"
    "ds_read_b128 v[120:123], v21 offset:1152\n"
    "v_and_b32 v22, 1, v3\n"
    "v_cmp_eq_u32 s[72:73], 1, v22\n"
    "v_lshlrev_b32 v14, 4, v22\n"
    "v_sub_u32 v23, v3, v22\n"
    "v_lshlrev_b32 v24, 2, v2\n"
    "v_sub_u32 v23, v23, v24\n"
    "v_mov_b32 v25, 0xff800000\n"
    "v_mov_b32 v26, 0\n"
    "v_cmp_le_i32 vcc, 0, v23\n"
    "s_nop 1\n"
    "v_cndmask_b32 v10, v25, v26, vcc\n"
    "v_cmp_le_i32 vcc, 2, v23\n"
    "s_nop 1\n"
    "v_cndmask_b32 v11, v25, v26, vcc\n"
    "v_cmp_ge_i32 vcc, -2, v23\n"
    "s_nop 1\n"
    "v_cndmask_b32 v8, v25, v26, vcc\n"
    "v_cmp_ge_i32 vcc, 0, v23\n"
    "s_nop 1\n"
    "v_cndmask_b32 v9, v25, v26, vcc\n"
    "s_or_b32 s60, s50, s57\n"
    "s_cmp_eq_u32 s60, 0\n"
    "s_cselect_b32 s60, 0xff800000, 0\n"
    "v_add_f32 v12, s60, v8\n"
    "v_add_f32 v13, s60, v9\n"
    "s_mov_b32 s78, 0x3eb8aa3b\n"
    "s_waitcnt lgkmcnt(0)\n"
    "s_barrier\n"
    "v_mov_b32_dpp v76, v40 row_newbcast:0 row_mask:0xf bank_mask:0xf\n"
    "s_add_u32 s64, s35, 0x6000\n"
    "v_mov_b32_dpp v77, v41 row_newbcast:0 row_mask:0xf bank_mask:0xf\n"
    "v_mov_b32_dpp v78, v42 row_newbcast:0 row_mask:0xf bank_mask:0xf\n"
    "v_mov_b32_dpp v79, v43 row_newbcast:0 row_mask:0xf bank_mask:0xf\n"
    "v_mov_b32 v80, v52\n"
    "v_mov_b32 v81, v52\n"
    "s_nop 1\n"
    "v_permlane16_swap_b32 v80, v81\n"
    "v_mov_b32 v81, v80\n"
    "s_nop 1\n"
    "v_permlane32_swap_b32 v80, v81\n"
    "v_pk_fma_f32 v[40:41], v[92:93], v[76:77], v[40:41] op_sel_hi:[0,1,1] neg_lo:[1,0,0] neg_hi:[1,0,0]\n"
    "v_pk_fma_f32 v[42:43], v[92:93], v[78:79], v[42:43] op_sel_hi:[0,1,1] neg_lo:[1,0,0] neg_hi:[1,0,0]\n"
    "s_add_u32 m0, s33, 0x1a000\n"
    "v_pk_mul_f32 v[40:41], v[40:41], v[92:93] op_sel:[0,1] op_sel_hi:[1,1]\n"
    "v_pk_mul_f32 v[42:43], v[42:43], v[92:93] op_sel:[0,1] op_sel_hi:[1,1]\n"
    "v_cvt_pk_bf16_f32 v128, v40, v41\n"
    "v_cvt_pk_bf16_f32 v129, v42, v43\n"
    "v_pk_fma_f32 v[44:45], v[94:95], v[76:77], v[44:45] op_sel_hi:[0,1,1] neg_lo:[1,0,0] neg_hi:[1,0,0]\n"
    "v_pk_fma_f32 v[46:47], v[94:95], v[78:79], v[46:47] op_sel_hi:[0,1,1] neg_lo:[1,0,0] neg_hi:[1,0,0]\n"
    "v_pk_mul_f32 v[44:45], v[44:45], v[94:95] op_sel:[0,1] op_sel_hi:[1,1]\n"
    "v_pk_mul_f32 v[46:47], v[46:47], v[94:95] op_sel:[0,1] op_sel_hi:[1,1]\n"
    "v_cvt_pk_bf16_f32 v130, v44, v45\n"
    "v_cvt_pk_bf16_f32 v131, v46, v47\n"
    "buffer_load_dwordx4 v1, s[36:39], s64 offen sc1 lds\n"
    "v_pk_fma_f32 v[48:49], v[96:97], v[76:77], v[48:49] op_sel_hi:[0,1,1] neg_lo:[1,0,0] neg_hi:[1,0,0]\n"
    "v_pk_fma_f32 v[50:51], v[96:97], v[78:79], v[50:51] op_sel_hi:[0,1,1] neg_lo:[1,0,0] neg_hi:[1,0,0]\n"
    "v_pk_mul_f32 v[48:49], v[48:49], v[96:97] op_sel:[0,1] op_sel_hi:[1,1]\n"
    "v_pk_mul_f32 v[50:51], v[50:51], v[96:97] op_sel:[0,1] op_sel_hi:[1,1]\n"
    "v_cvt_pk_bf16_f32 v132, v48, v49\n"
    "v_cvt_pk_bf16_f32 v133, v50, v51\n"
    "v_pk_fma_f32 v[64:65], v[94:95], v[72:73], v[64:65] op_sel_hi:[0,1,1] neg_lo:[1,0,0] neg_hi:[1,0,0]\n"
    "v_pk_fma_f32 v[66:67], v[94:95], v[74:75], v[66:67] op_sel_hi:[0,1,1] neg_lo:[1,0,0] neg_hi:[1,0,0]\n"
    "v_pk_mul_f32 v[64:65], v[64:65], v[94:95] op_sel:[0,1] op_sel_hi:[1,1]\n"
    "v_pk_mul_f32 v[66:67], v[66:67], v[94:95] op_sel:[0,1] op_sel_hi:[1,1]\n"
    "s_add_u32 s64, s35, 0x8000\n"
    "v_cvt_pk_bf16_f32 v124, v64, v65\n"
    "v_cvt_pk_bf16_f32 v125, v66, v67\n"
    "v_pk_fma_f32 v[68:69], v[96:97], v[72:73], v[68:69] op_sel_hi:[0,1,1] neg_lo:[1,0,0] neg_hi:[1,0,0]\n"
    "v_pk_fma_f32 v[70:71], v[96:97], v[74:75], v[70:71] op_sel_hi:[0,1,1] neg_lo:[1,0,0] neg_hi:[1,0,0]\n"
    "v_pk_mul_f32 v[68:69], v[68:69], v[96:97] op_sel:[0,1] op_sel_hi:[1,1]\n"
    "v_pk_mul_f32 v[70:71], v[70:71], v[96:97] op_sel:[0,1] op_sel_hi:[1,1]\n"
    "v_cvt_pk_bf16_f32 v126, v68, v69\n"
    "v_cvt_pk_bf16_f32 v127, v70, v71\n"
    "v_mfma_f32_16x16x16_bf16 v[140:143], v[128:129], v[124:125], 0\n"
    "v_mfma_f32_16x16x16_bf16 v[144:147], v[130:131], v[124:125], 0\n"
    "s_add_u32 m0, s33, 0x1c000\n"
    "v_mfma_f32_16x16x16_bf16 v[148:151], v[130:131], v[126:127], 0\n"
    "v_mfma_f32_16x16x16_bf16 v[152:155], v[132:133], v[126:127], 0\n"
    "v_pk_fma_f32 v[52:53], v[100:101], v[80:81], v[52:53] op_sel_hi:[1,0,1] neg_lo:[1,0,0] neg_hi:[1,0,0]\n"
    "v_pk_fma_f32 v[54:55], v[102:103], v[80:81], v[54:55] op_sel_hi:[1,0,1] neg_lo:[1,0,0] neg_hi:[1,0,0]\n"
    "v_pk_mul_f32 v[52:53], v[52:53], v[112:113]\n"
    "v_pk_mul_f32 v[54:55], v[54:55], v[114:115]\n"
    "v_cvt_pk_bf16_f32 v134, v52, v53\n"
    "v_cvt_pk_bf16_f32 v135, v54, v55\n"
    "v_pk_fma_f32 v[56:57], v[104:105], v[80:81], v[56:57] op_sel_hi:[1,0,1] neg_lo:[1,0,0] neg_hi:[1,0,0]\n"
    "v_pk_fma_f32 v[58:59], v[106:107], v[80:81], v[58:59] op_sel_hi:[1,0,1] neg_lo:[1,0,0] neg_hi:[1,0,0]\n"
    "buffer_load_dwordx4 v1, s[36:39], s64 offen sc1 lds\n"
    "v_pk_mul_f32 v[56:57], v[56:57], v[116:117]\n"
    "v_pk_mul_f32 v[58:59], v[58:59], v[118:119]\n"
    "v_cvt_pk_bf16_f32 v136, v56, v57\n"
    "v_cvt_pk_bf16_f32 v137, v58, v59\n"
    "v_pk_fma_f32 v[60:61], v[108:109], v[80:81], v[60:61] op_sel_hi:[1,0,1] neg_lo:[1,0,0] neg_hi:[1,0,0]\n"
    "v_pk_fma_f32 v[62:63], v[110:111], v[80:81], v[62:63] op_sel_hi:[1,0,1] neg_lo:[1,0,0] neg_hi:[1,0,0]\n"
    "v_pk_mul_f32 v[60:61], v[60:61], v[120:121]\n"
    "v_pk_mul_f32 v[62:63], v[62:63], v[122:123]\n"
    "v_cvt_pk_bf16_f32 v138, v60, v61\n"
    "v_cvt_pk_bf16_f32 v139, v62, v63\n"
    "s_add_u32 s64, s35, 0xa000\n"
    "v_cndmask_b32 v40, v140, v141, s[72:73]\n"
    "v_cndmask_b32 v48, v148, v149, s[72:73]\n"
    "v_cndmask_b32 v41, v142, v143, s[72:73]\n"
    "v_cndmask_b32 v49, v150, v151, s[72:73]\n"
    "v_cndmask_b32 v42, v144, v145, s[72:73]\n"
    "v_cndmask_b32 v50, v152, v153, s[72:73]\n"
    "v_cndmask_b32 v43, v146, v147, s[72:73]\n"
    "v_cndmask_b32 v51, v154, v155, s[72:73]\n"
    "v_fma_f32 v40, v40, s78, v12\n"
    "v_fma_f32 v48, v48, s78, v8\n"
    "s_add_u32 m0, s33, 0x1e000\n"
    "v_fma_f32 v41, v41, s78, v13\n"
    "v_fma_f32 v49, v49, s78, v9\n"
    "v_fma_f32 v42, v42, s78, v10\n"
    "v_fma_f32 v50, v50, s78, v10\n"
    "v_fma_f32 v43, v43, s78, v11\n"
    "v_fma_f32 v51, v51, s78, v11\n"
    "v_max3_f32 v44, v40, v41, v42\n"
    "v_max3_f32 v52, v48, v49, v50\n"
    "v_max_f32 v44, v44, v43\n"
    "v_max_f32 v52, v52, v51\n"
    "buffer_load_dwordx4 v1, s[36:39], s64 offen sc1 lds\n"
    "v_mov_b32 v45, v44\n"
    "v_mov_b32 v53, v52\n"
    "s_nop 0\n"
    "v_permlane16_swap_b32 v44, v45\n"
    "v_permlane16_swap_b32 v52, v53\n"
    "v_max_f32 v44, v44, v45\n"
    "v_max_f32 v52, v52, v53\n"
    "v_mov_b32 v45, v44\n"
    "v_mov_b32 v53, v52\n"
    "s_nop 0\n"
    "v_permlane32_swap_b32 v44, v45\n"
    "v_permlane32_swap_b32 v52, v53\n"
    "buffer_load_dwordx4 v[160:163], v1, s[36:39], s66 offen sc1\n"
    "v_max_f32 v44, v44, v45\n"
    "v_max_f32 v52, v52, v53\n"
    "v_sub_f32 v40, v40, v44\n"
    "v_sub_f32 v48, v48, v52\n"
    "v_sub_f32 v41, v41, v44\n"
    "v_sub_f32 v49, v49, v52\n"
    "v_sub_f32 v42, v42, v44\n"
    "v_sub_f32 v50, v50, v52\n"
    "v_sub_f32 v43, v43, v44\n"
    "v_sub_f32 v51, v51, v52\n"
    "buffer_load_dwordx4 v[164:167], v1, s[36:39], s66 offen offset:1024 sc1\n"
    "v_exp_f32 v40, v40\n"
    "v_exp_f32 v48, v48\n"
    "v_exp_f32 v41, v41\n"
    "v_exp_f32 v49, v49\n"
    "v_exp_f32 v42, v42\n"
    "v_exp_f32 v50, v50\n"
    "v_exp_f32 v43, v43\n"
    "v_exp_f32 v51, v51\n"
    "v_cvt_pk_bf16_f32 v64, v40, 0\n"
    "v_cvt_pk_bf16_f32 v68, v48, 0\n"
    "v_cvt_pk_bf16_f32 v65, v41, 0\n"
    "v_cvt_pk_bf16_f32 v69, v49, 0\n"
    "v_cvt_pk_bf16_f32 v66, v42, 0\n"
    "v_cvt_pk_bf16_f32 v70, v50, 0\n"
    "v_cvt_pk_bf16_f32 v67, v43, 0\n"
    "v_cvt_pk_bf16_f32 v71, v51, 0\n"
    "v_lshlrev_b32 v64, v14, v64\n"
    "v_lshlrev_b32 v68, v14, v68\n"
    "v_lshlrev_b32 v65, v14, v65\n"
    "v_lshlrev_b32 v69, v14, v69\n"
    "v_lshlrev_b32 v66, v14, v66\n"
    "v_lshlrev_b32 v70, v14, v70\n"
    "v_lshlrev_b32 v67, v14, v67\n"
    "v_lshlrev_b32 v71, v14, v71\n"
    "s_nop 0\n"
    "v_mfma_f32_16x16x32_bf16 v[72:75], v[134:137], v[64:67], 0\n"
    "v_mfma_f32_16x16x32_bf16 v[76:79], v[136:139], v[68:71], 0\n"
    "v_add_f32 v44, v40, v41\n"
    "v_add_f32 v52, v48, v49\n"
    "v_add_f32 v45, v42, v43\n"
    "v_add_f32 v53, v50, v51\n"
    "v_add_f32 v44, v44, v45\n"
    "v_add_f32 v52, v52, v53\n"
    "v_mov_b32 v45, v44\n"
    "v_mov_b32 v53, v52\n"
    "s_nop 0\n"
    "v_permlane16_swap_b32 v44, v45\n"
    "v_permlane16_swap_b32 v52, v53\n"
    "v_add_f32 v44, v44, v45\n"
    "v_add_f32 v52, v52, v53\n"
    "v_mov_b32 v45, v44\n"
    "v_mov_b32 v53, v52\n"
    "s_nop 0\n"
    "v_permlane32_swap_b32 v44, v45\n"
    "v_permlane32_swap_b32 v52, v53\n"
    "v_add_f32 v44, v44, v45\n"
    "v_add_f32 v52, v52, v53\n"
    "v_rcp_f32 v44, v44\n"
    "v_rcp_f32 v52, v52\n"
    "v_mul_f32 v72, v72, v44\n"
    "v_mul_f32 v76, v76, v52\n"
    "v_mul_f32 v73, v73, v44\n"
    "v_mul_f32 v77, v77, v52\n"
    "v_mul_f32 v74, v74, v44\n"
    "v_mul_f32 v78, v78, v52\n"
    "v_mul_f32 v75, v75, v44\n"
    "v_mul_f32 v79, v79, v52\n"
    "v_cvt_pk_bf16_f32 v156, v72, v73\n"
    "v_cvt_pk_bf16_f32 v158, v76, v77\n"
    "v_cvt_pk_bf16_f32 v157, v74, v75\n"
    "v_cvt_pk_bf16_f32 v159, v78, v79\n"
    "v_and_b32 v20, 1, v2\n"
    "v_lshlrev_b32 v20, 14, v20\n"
    "v_lshrrev_b32 v21, 1, v2\n"
    "v_lshl_add_u32 v21, v21, 4, v3\n"
    "v_lshl_add_u32 v20, v21, 4, v20\n"
    "s_lshl_b32 s60, s51, 2\n"
    "s_add_u32 s60, s60, s53\n"
    "s_lshl_b32 s60, s60, 9\n"
    "s_lshl_b32 s61, s50, 1\n"
    "s_add_u32 s61, s61, s57\n"
    "s_lshl_b32 s61, s61, 15\n"
    "s_add_u32 s60, s60, s61\n"
    "s_add_u32 s60, s60, 0x488000\n"
    "v_permlane16_swap_b32 v156, v158\n"
    "v_permlane16_swap_b32 v157, v159\n"
    "buffer_store_dwordx4 v[156:159], v20, s[36:39], s60 offen sc1\n"
    "buffer_load_dwordx4 v[168:171], v1, s[36:39], s66 offen offset:2048 sc1\n"
    "buffer_load_dwordx4 v[172:175], v1, s[36:39], s66 offen offset:3072 sc1\n"
    "s_add_u32 s66, s66, 0x1000\n"
    "buffer_load_dwordx4 v[176:179], v1, s[36:39], s66 offen sc1\n"
    "buffer_load_dwordx4 v[180:183], v1, s[36:39], s66 offen offset:1024 sc1\n"
    "buffer_load_dwordx4 v[184:187], v1, s[36:39], s66 offen offset:2048 sc1\n"
    "buffer_load_dwordx4 v[188:191], v1, s[36:39], s66 offen offset:3072 sc1\n"
    "s_or_b32 s60, s48, s49\n"
    "s_cmp_eq_u32 s60, 0\n"
    "s_cbranch_scc0 .Lep_done\n"
    "v_mov_b32 v22, s56\n"
    "v_mov_b32 v21, 0\n"
    "s_mov_b32 s60, 0xe88000\n"
    "s_mov_b64 exec, 1\n"
    "buffer_store_dword v22, v21, s[36:39], s60 offen sc1\n"
    "s_mov_b64 exec, -1\n"
    ".Lep_done:\n"
    "s_mul_i32 s60, s57, 0x8000\n"
    "s_add_u32 s60, s60, 0x14000\n"
    "s_lshl_b32 s61, s57, 11\n"
    "s_add_u32 s62, s60, s61\n"
    "v_add_u32 v8, s62, v1\n"
    "s_xor_b32 s61, s61, 0x800\n"
    "s_add_u32 s62, s60, s61\n"
    "v_add_u32 v9, s62, v1\n"
    "s_lshl_b32 s60, s53, 11\n"
    "s_add_u32 s60, s60, 0x10000\n"
    "s_mul_i32 s61, s57, 0x2000\n"
    "s_add_u32 s62, s60, s61\n"
    "v_add_u32 v11, s62, v1\n"
    "s_sub_u32 s61, 0x2000, s61\n"
    "s_add_u32 s62, s60, s61\n"
    "v_add_u32 v10, s62, v1\n"
    "s_lshl_b32 s69, s50, 2\n"
    "s_add_u32 s69, s69, s53\n"
    "s_lshl_b32 s69, s69, 1\n"
    "s_add_u32 s69, s69, s57\n"
    "s_lshl_b32 s69, s69, 13\n"
    "s_add_u32 s69, s69, 0x488000\n"
    "s_add_u32 s70, s69, 0x1000\n"
    "s_mov_b32 s76, 0\n"
    "s_waitcnt vmcnt(6)\n"
    "s_barrier\n"
    "buffer_load_dwordx4 v[40:43], v1, s[36:39], s69 offen sc1\n"
    "buffer_load_dwordx4 v[44:47], v1, s[36:39], s69 offen offset:1024 sc1\n"
    "buffer_load_dwordx4 v[48:51], v1, s[36:39], s69 offen offset:2048 sc1\n"
    "buffer_load_dwordx4 v[52:55], v1, s[36:39], s69 offen offset:3072 sc1\n"
    "buffer_load_dwordx4 v[56:59], v1, s[36:39], s70 offen sc1\n"
    "buffer_load_dwordx4 v[60:63], v1, s[36:39], s70 offen offset:1024 sc1\n"
    "buffer_load_dwordx4 v[64:67], v1, s[36:39], s70 offen offset:2048 sc1\n"
    "buffer_load_dwordx4 v[68:71], v1, s[36:39], s70 offen offset:3072 sc1\n"
    ".Lp2_mm:\n"
    "ds_read_b128 v[88:91], v8\n"
    "ds_read_b128 v[92:95], v8 offset:1024\n"
    "ds_read_b128 v[96:99], v9\n"
    "ds_read_b128 v[100:103], v9 offset:1024\n"
    "ds_read_b128 v[104:107], v8 offset:4096\n"
    "ds_read_b128 v[108:111], v8 offset:5120\n"
    "v_lshlrev_b32 v72, 16, v4\n"
    "v_and_b32 v73, 0xffff0000, v4\n"
    "v_lshlrev_b32 v74, 16, v5\n"
    "v_and_b32 v75, 0xffff0000, v5\n"
    "v_lshlrev_b32 v76, 16, v6\n"
    "v_and_b32 v77, 0xffff0000, v6\n"
    "v_lshlrev_b32 v78, 16, v7\n"
    "v_and_b32 v79, 0xffff0000, v7\n"
    "s_waitcnt vmcnt(7) lgkmcnt(5)\n"
    "v_mfma_f32_16x16x32_bf16 v[72:75], v[88:91], v[40:43], v[72:75]\n"
    "ds_read_b128 v[112:115], v9 offset:4096\n"
    "s_waitcnt lgkmcnt(5)\n"
    "v_mfma_f32_16x16x32_bf16 v[76:79], v[92:95], v[40:43], v[76:79]\n"
    "buffer_load_dwordx4 v[192:195], v1, s[36:39], s67 offen sc1\n"
    "ds_read_b128 v[116:119], v9 offset:5120\n"
    "s_waitcnt lgkmcnt(5)\n"
    "v_mfma_f32_16x16x32_bf16 v[80:83], v[96:99], v[40:43], 0\n"
    "ds_read_b128 v[88:91], v8 offset:8192\n"
    "s_waitcnt lgkmcnt(5)\n"
    "v_mfma_f32_16x16x32_bf16 v[84:87], v[100:103], v[40:43], 0\n"
    "ds_read_b128 v[92:95], v8 offset:9216\n"
    "s_waitcnt vmcnt(7) lgkmcnt(5)\n"
    "v_mfma_f32_16x16x32_bf16 v[72:75], v[104:107], v[44:47], v[72:75]\n"
    "ds_read_b128 v[96:99], v9 offset:8192\n"
    "s_waitcnt lgkmcnt(5)\n"
    "v_mfma_f32_16x16x32_bf16 v[76:79], v[108:111], v[44:47], v[76:79]\n"
    "buffer_load_dwordx4 v[196:199], v1, s[36:39], s67 offen offset:1024 sc1\n"
    "ds_read_b128 v[100:103], v9 offset:9216\n"
    "s_waitcnt lgkmcnt(5)\n"
    "v_mfma_f32_16x16x32_bf16 v[80:83], v[112:115], v[44:47], v[80:83]\n"
    "ds_read_b128 v[104:107], v8 offset:12288\n"
    "s_waitcnt lgkmcnt(5)\n"
    "v_mfma_f32_16x16x32_bf16 v[84:87], v[116:119], v[44:47], v[84:87]\n"
    "ds_read_b128 v[108:111], v8 offset:13312\n"
    "s_waitcnt vmcnt(7) lgkmcnt(5)\n"
    "v_mfma_f32_16x16x32_bf16 v[72:75], v[88:91], v[48:51], v[72:75]\n"
    "ds_read_b128 v[112:115], v9 offset:12288\n"
    "s_waitcnt lgkmcnt(5)\n"
    "v_mfma_f32_16x16x32_bf16 v[76:79], v[92:95], v[48:51], v[76:79]\n"
    "buffer_load_dwordx4 v[200:203], v1, s[36:39], s67 offen offset:2048 sc1\n"
    "ds_read_b128 v[116:119], v9 offset:13312\n"
    "s_waitcnt lgkmcnt(5)\n"
    "v_mfma_f32_16x16x32_bf16 v[80:83], v[96:99], v[48:51], v[80:83]\n"
    "ds_read_b128 v[88:91], v8 offset:16384\n"
    "s_waitcnt lgkmcnt(5)\n"
    "v_mfma_f32_16x16x32_bf16 v[84:87], v[100:103], v[48:51], v[84:87]\n"
    "ds_read_b128 v[92:95], v8 offset:17408\n"
    "s_waitcnt vmcnt(7) lgkmcnt(5)\n"
    "v_mfma_f32_16x16x32_bf16 v[72:75], v[104:107], v[52:55], v[72:75]\n"
    "ds_read_b128 v[96:99], v9 offset:16384\n"
    "s_waitcnt lgkmcnt(5)\n"
    "v_mfma_f32_16x16x32_bf16 v[76:79], v[108:111], v[52:55], v[76:79]\n"
    "buffer_load_dwordx4 v[204:207], v1, s[36:39], s67 offen offset:3072 sc1\n"
    "ds_read_b128 v[100:103], v9 offset:17408\n"
    "s_waitcnt lgkmcnt(5)\n"
    "v_mfma_f32_16x16x32_bf16 v[80:83], v[112:115], v[52:55], v[80:83]\n"
    "ds_read_b128 v[104:107], v8 offset:20480\n"
    "s_waitcnt lgkmcnt(5)\n"
    "v_mfma_f32_16x16x32_bf16 v[84:87], v[116:119], v[52:55], v[84:87]\n"
    "ds_read_b128 v[108:111], v8 offset:21504\n"
    "s_waitcnt vmcnt(7) lgkmcnt(5)\n"
    "v_mfma_f32_16x16x32_bf16 v[72:75], v[88:91], v[56:59], v[72:75]\n"
    "ds_read_b128 v[112:115], v9 offset:20480\n"
    "s_waitcnt lgkmcnt(5)\n"
    "v_mfma_f32_16x16x32_bf16 v[76:79], v[92:95], v[56:59], v[76:79]\n"
    "s_add_u32 s67, s67, 0x1000\n"
    "ds_read_b128 v[116:119], v9 offset:21504\n"
    "s_waitcnt lgkmcnt(5)\n"
    "v_mfma_f32_16x16x32_bf16 v[80:83], v[96:99], v[56:59], v[80:83]\n"
    "ds_read_b128 v[88:91], v8 offset:24576\n"
    "s_waitcnt lgkmcnt(5)\n"
    "v_mfma_f32_16x16x32_bf16 v[84:87], v[100:103], v[56:59], v[84:87]\n"
    "ds_read_b128 v[92:95], v8 offset:25600\n"
    "s_waitcnt vmcnt(6) lgkmcnt(5)\n"
    "v_mfma_f32_16x16x32_bf16 v[72:75], v[104:107], v[60:63], v[72:75]\n"
    "ds_read_b128 v[96:99], v9 offset:24576\n"
    "s_waitcnt lgkmcnt(5)\n"
    "v_mfma_f32_16x16x32_bf16 v[76:79], v[108:111], v[60:63], v[76:79]\n"
    "buffer_load_dwordx4 v[208:211], v1, s[36:39], s67 offen sc1\n"
    "ds_read_b128 v[100:103], v9 offset:25600\n"
    "s_waitcnt lgkmcnt(5)\n"
    "v_mfma_f32_16x16x32_bf16 v[80:83], v[112:115], v[60:63], v[80:83]\n"
    "ds_read_b128 v[104:107], v8 offset:28672\n"
    "s_waitcnt lgkmcnt(5)\n"
    "v_mfma_f32_16x16x32_bf16 v[84:87], v[116:119], v[60:63], v[84:87]\n"
    "ds_read_b128 v[108:111], v8 offset:29696\n"
    "s_waitcnt vmcnt(6) lgkmcnt(5)\n"
    "v_mfma_f32_16x16x32_bf16 v[72:75], v[88:91], v[64:67], v[72:75]\n"
    "ds_read_b128 v[112:115], v9 offset:28672\n"
    "s_waitcnt lgkmcnt(5)\n"
    "v_mfma_f32_16x16x32_bf16 v[76:79], v[92:95], v[64:67], v[76:79]\n"
    "buffer_load_dwordx4 v[212:215], v1, s[36:39], s67 offen offset:1024 sc1\n"
    "ds_read_b128 v[116:119], v9 offset:29696\n"
    "s_waitcnt lgkmcnt(5)\n"
    "v_mfma_f32_16x16x32_bf16 v[80:83], v[96:99], v[64:67], v[80:83]\n"
    "s_waitcnt lgkmcnt(4)\n"
    "v_mfma_f32_16x16x32_bf16 v[84:87], v[100:103], v[64:67], v[84:87]\n"
    "s_waitcnt vmcnt(6) lgkmcnt(3)\n"
    "v_mfma_f32_16x16x32_bf16 v[72:75], v[104:107], v[68:71], v[72:75]\n"
    "s_waitcnt lgkmcnt(2)\n"
    "v_mfma_f32_16x16x32_bf16 v[76:79], v[108:111], v[68:71], v[76:79]\n"
    "buffer_load_dwordx4 v[216:219], v1, s[36:39], s67 offen offset:2048 sc1\n"
    "s_waitcnt lgkmcnt(1)\n"
    "v_mfma_f32_16x16x32_bf16 v[80:83], v[112:115], v[68:71], v[80:83]\n"
    "s_waitcnt lgkmcnt(0)\n"
    "v_mfma_f32_16x16x32_bf16 v[84:87], v[116:119], v[68:71], v[84:87]\n"
    "buffer_load_dwordx4 v[220:223], v1, s[36:39], s67 offen offset:3072 sc1\n"
    "s_nop 4\n"
    "ds_write_b128 v10, v[80:83]\n"
    "s_nop 0\n"
    "ds_write_b128 v10, v[84:87] offset:1024\n"
    "v_cmp_u_f32 vcc, v84, v84\n"
    "s_cmp_eq_u32 s76, 0\n"
    "s_cselect_b64 vcc, vcc, 0\n"
    "s_cbranch_vccnz .Lp2_slow\n"
    ".Lp2_cont:\n"
    "s_waitcnt lgkmcnt(0)\n"
    "s_barrier\n"
    "ds_read_b128 v[120:123], v11\n"
    "ds_read_b128 v[124:127], v11 offset:1024\n"
    "s_waitcnt lgkmcnt(1)\n"
    "v_add_f32 v72, v72, v120\n"
    "v_add_f32 v73, v73, v121\n"
    "v_add_f32 v74, v74, v122\n"
    "v_add_f32 v75, v75, v123\n"
    "v_cvt_pk_bf16_f32 v4, v72, v73\n"
    "v_cvt_pk_bf16_f32 v5, v74, v75\n"
    "s_waitcnt lgkmcnt(0)\n"
    "v_add_f32 v76, v76, v124\n"
    "v_add_f32 v77, v77, v125\n"
    "v_add_f32 v78, v78, v126\n"
    "v_add_f32 v79, v79, v127\n"
    "v_cvt_pk_bf16_f32 v6, v76, v77\n"
    "v_cvt_pk_bf16_f32 v7, v78, v79\n"
    "s_lshl_b32 s60, s50, 2\n"
    "s_add_u32 s60, s60, s53\n"
    "s_lshl_b32 s62, s51, 1\n"
    "s_add_u32 s62, s62, s57\n"
    "s_lshl_b32 s61, s60, 4\n"
    "s_add_u32 s61, s61, s62\n"
    "s_lshl_b32 s60, s61, 10\n"
    "s_add_u32 s60, s60, 0x688000\n"
    "s_lshl_b32 s61, s61, 7\n"
    "s_add_u32 s61, s61, 0xde8000\n"
    "buffer_store_dwordx4 v[4:7], v1, s[36:39], s60 offen sc1\n"
    "v_mov_b32 v16, 0\n"
    "v_mov_b32 v17, 0\n"
    "v_mov_b32 v18, 0\n"
    "v_mov_b32 v19, 0\n"
    "v_dot2c_f32_bf16 v16, 0x3f803f80, v4\n"
    "v_dot2c_f32_bf16 v17, v4, v4\n"
    "v_dot2c_f32_bf16 v18, 0x3f803f80, v5\n"
    "v_dot2c_f32_bf16 v19, v5, v5\n"
    "v_dot2c_f32_bf16 v16, 0x3f803f80, v6\n"
    "v_dot2c_f32_bf16 v17, v6, v6\n"
    "v_dot2c_f32_bf16 v18, 0x3f803f80, v7\n"
    "v_dot2c_f32_bf16 v19, v7, v7\n"
    "v_lshlrev_b32 v20, 3, v3\n"
    "s_nop 0\n"
    "v_add_f32 v16, v16, v18\n"
    "v_add_f32 v17, v17, v19\n"
    "v_mov_b32 v18, v16\n"
    "v_mov_b32 v19, v17\n"
    "s_nop 0\n"
    "v_permlane16_swap_b32 v16, v18\n"
    "v_permlane16_swap_b32 v17, v19\n"
    "v_add_f32 v16, v16, v18\n"
    "v_add_f32 v17, v17, v19\n"
    "v_mov_b32 v18, v16\n"
    "v_mov_b32 v19, v17\n"
    "s_nop 0\n"
    "v_permlane32_swap_b32 v16, v18\n"
    "v_permlane32_swap_b32 v17, v19\n"
    "v_add_f32 v16, v16, v18\n"
    "v_add_f32 v17, v17, v19\n"
    "s_mov_b64 exec, 0xffff\n"
    "buffer_store_dwordx2 v[16:17], v20, s[36:39], s61 offen sc1\n"
    "s_mov_b64 exec, -1\n"
    "s_mul_i32 s70, s51, 0x2b000\n"
    "s_lshl_b32 s60, s53, 10\n"
    "s_add_u32 s70, s70, s60\n"
    "s_add_u32 s70, s70, 0x330000\n"
    "s_mov_b32 s33, s60\n"
    "s_mul_i32 s60, s57, 0x16000\n"
    "s_add_u32 s71, s70, s60\n"
    "s_mul_i32 s60, s57, 0x9000\n"
    "s_add_u32 s35, s33, s60\n"
    "v_mov_b32 v24, 0x3f803f80\n"
    "v_mov_b32 v25, 0x3f803f80\n"
    "v_mov_b32 v26, 0x3f803f80\n"
    "v_mov_b32 v27, 0x3f803f80\n"
    "s_mul_i32 s60, s53, 0xc00\n"
    "v_add_u32 v8, s60, v1\n"
    "s_mul_i32 s60, s57, 0x6000\n"
    "s_add_u32 s60, s60, 0x6800\n"
    "v_add_u32 v9, s60, v1\n"
    "s_cmp_eq_u32 s57, 0\n"
    "s_nop 1\n"
    "s_cbranch_scc0 .Lp3_fin\n"
    "buffer_load_dwordx4 v[224:227], v1, s[36:39], s68 offen sc1\n"
    "buffer_load_dwordx4 v[228:231], v1, s[36:39], s68 offen offset:1024 sc1\n"
    "buffer_load_dwordx4 v[232:235], v1, s[36:39], s68 offen offset:2048 sc1\n"
    "buffer_load_dwordx4 v[236:239], v1, s[36:39], s68 offen offset:3072 sc1\n"
    "s_add_u32 s68, s68, 0x1000\n"
    "buffer_load_dwordx4 v[240:243], v1, s[36:39], s68 offen sc1\n"
    "buffer_load_dwordx4 v[244:247], v1, s[36:39], s68 offen offset:1024 sc1\n"
    "buffer_load_dwordx4 v[248:251], v1, s[36:39], s68 offen offset:2048 sc1\n"
    "buffer_load_dwordx4 v[252:255], v1, s[36:39], s68 offen offset:3072 sc1\n"
    "s_add_u32 s64, s71, 0\n"
    "s_add_u32 m0, s35, 0x14800\n"
    "s_nop 0\n"
    "buffer_load_dwordx4 v1, s[36:39], s64 offen sc1 lds\n"
    "s_add_u32 s64, s71, 0x1000\n"
    "s_add_u32 m0, s35, 0x15800\n"
    "s_nop 0\n"
    "buffer_load_dwordx4 v1, s[36:39], s64 offen sc1 lds\n"
    "s_add_u32 s64, s71, 0x2000\n"
    "s_add_u32 m0, s35, 0x16800\n"
    "s_nop 0\n"
    "buffer_load_dwordx4 v1, s[36:39], s64 offen sc1 lds\n"
    "s_setprio 1\n"
    "s_lshl_b32 s69, s52, 17\n"
    "s_lshl_b32 s60, s53, 11\n"
    "s_add_u32 s69, s69, s60\n"
    "s_add_u32 s69, s69, 0x688000\n"
    "s_add_u32 s60, s60, 0x6800\n"
    "v_add_u32 v10, s60, v1\n"
    "s_waitcnt vmcnt(11)\n"
    "s_add_u32 s65, s69, 0\n"
    "buffer_load_dwordx4 v[64:67], v1, s[36:39], s65 offen sc1\n"
    "buffer_load_dwordx4 v[68:71], v1, s[36:39], s65 offen offset:1024 sc1\n"
    "s_add_u32 s65, s69, 0x4000\n"
    "buffer_load_dwordx4 v[72:75], v1, s[36:39], s65 offen sc1\n"
    "buffer_load_dwordx4 v[76:79], v1, s[36:39], s65 offen offset:1024 sc1\n"
    "s_add_u32 s65, s69, 0x2000\n"
    "buffer_load_dwordx4 v[80:83], v1, s[36:39], s65 offen sc1\n"
    "buffer_load_dwordx4 v[84:87], v1, s[36:39], s65 offen offset:1024 sc1\n"
    "s_add_u32 s65, s69, 0x8000\n"
    "buffer_load_dwordx4 v[88:91], v1, s[36:39], s65 offen sc1\n"
    "buffer_load_dwordx4 v[92:95], v1, s[36:39], s65 offen offset:1024 sc1\n"
    "s_add_u32 s65, s69, 0x6000\n"
    "buffer_load_dwordx4 v[96:99], v1, s[36:39], s65 offen sc1\n"
    "buffer_load_dwordx4 v[100:103], v1, s[36:39], s65 offen offset:1024 sc1\n"
    "v_mfma_f32_16x16x32_bf16 v[12:15], v[160:163], v[24:27], 0\n"
    "v_mfma_f32_16x16x32_bf16 v[12:15], v[164:167], v[24:27], v[12:15]\n"
    "v_mfma_f32_16x16x32_bf16 v[12:15], v[168:171], v[24:27], v[12:15]\n"
    "v_mfma_f32_16x16x32_bf16 v[12:15], v[172:175], v[24:27], v[12:15]\n"
    "v_mfma_f32_16x16x32_bf16 v[12:15], v[176:179], v[24:27], v[12:15]\n"
    "v_mfma_f32_16x16x32_bf16 v[12:15], v[180:183], v[24:27], v[12:15]\n"
    "v_mfma_f32_16x16x32_bf16 v[12:15], v[184:187], v[24:27], v[12:15]\n"
    "v_mfma_f32_16x16x32_bf16 v[12:15], v[188:191], v[24:27], v[12:15]\n"
    "v_mfma_f32_16x16x32_bf16 v[16:19], v[192:195], v[24:27], 0\n"
    "v_mfma_f32_16x16x32_bf16 v[16:19], v[196:199], v[24:27], v[16:19]\n"
    "v_mfma_f32_16x16x32_bf16 v[16:19], v[200:203], v[24:27], v[16:19]\n"
    "v_mfma_f32_16x16x32_bf16 v[16:19], v[204:207], v[24:27], v[16:19]\n"
    "v_mfma_f32_16x16x32_bf16 v[16:19], v[208:211], v[24:27], v[16:19]\n"
    "v_mfma_f32_16x16x32_bf16 v[16:19], v[212:215], v[24:27], v[16:19]\n"
    "v_mfma_f32_16x16x32_bf16 v[16:19], v[216:219], v[24:27], v[16:19]\n"
    "v_mfma_f32_16x16x32_bf16 v[16:19], v[220:223], v[24:27], v[16:19]\n"
    "s_waitcnt vmcnt(20)\n"
    "v_mfma_f32_16x16x32_bf16 v[20:23], v[224:227], v[24:27], 0\n"
    "s_waitcnt vmcnt(19)\n"
    "v_mfma_f32_16x16x32_bf16 v[20:23], v[228:231], v[24:27], v[20:23]\n"
    "s_waitcnt vmcnt(18)\n"
    "v_mfma_f32_16x16x32_bf16 v[20:23], v[232:235], v[24:27], v[20:23]\n"
    "s_waitcnt vmcnt(17)\n"
    "v_mfma_f32_16x16x32_bf16 v[20:23], v[236:239], v[24:27], v[20:23]\n"
    "s_waitcnt vmcnt(16)\n"
    "v_mfma_f32_16x16x32_bf16 v[20:23], v[240:243], v[24:27], v[20:23]\n"
    "s_waitcnt vmcnt(15)\n"
    "v_mfma_f32_16x16x32_bf16 v[20:23], v[244:247], v[24:27], v[20:23]\n"
    "s_waitcnt vmcnt(14)\n"
    "v_mfma_f32_16x16x32_bf16 v[20:23], v[248:251], v[24:27], v[20:23]\n"
    "s_waitcnt vmcnt(13)\n"
    "v_mfma_f32_16x16x32_bf16 v[20:23], v[252:255], v[24:27], v[20:23]\n"
    "ds_write_b128 v8, v[12:15] offset:12288\n"
    "ds_write_b128 v8, v[16:19] offset:13312\n"
    "s_nop 5\n"
    "ds_write_b128 v8, v[20:23] offset:14336\n"
    "s_waitcnt vmcnt(9)\n"
    "v_max3_u32 v11, v64, v65, v66\n"
    "s_waitcnt vmcnt(8)\n"
    "v_max3_u32 v11, v11, v67, v68\n"
    "v_max3_u32 v11, v11, v69, v70\n"
    "v_max_u32 v11, v11, v71\n"
    "v_cmp_eq_u32 vcc, -1, v11\n"
    "s_nop 0\n"
    "s_cbranch_vccnz .Lh_slow0\n"
    ".Lh_ok0:\n"
    "ds_write_b128 v10, v[64:67]\n"
    "ds_write_b128 v10, v[68:71] offset:1024\n"
    "s_waitcnt lgkmcnt(0)\n"
    "s_barrier\n"
    "ds_read_b128 v[28:31], v9\n"
    "ds_read_b128 v[32:35], v9 offset:1024\n"
    "ds_read_b128 v[36:39], v9 offset:2048\n"
    "ds_read_b128 v[40:43], v9 offset:3072\n"
    "s_waitcnt lgkmcnt(3)\n"
    "v_mfma_f32_16x16x32_bf16 v[52:55], v[160:163], v[28:31], 0\n"
    "s_waitcnt vmcnt(7)\n"
    "v_max3_u32 v11, v72, v73, v74\n"
    "s_waitcnt vmcnt(6)\n"
    "v_max3_u32 v11, v11, v75, v76\n"
    "v_mfma_f32_16x16x32_bf16 v[56:59], v[192:195], v[28:31], 0\n"
    "v_max3_u32 v11, v11, v77, v78\n"
    "v_max_u32 v11, v11, v79\n"
    "v_mfma_f32_16x16x32_bf16 v[60:63], v[224:227], v[28:31], 0\n"
    "v_cmp_eq_u32 vcc, -1, v11\n"
    "s_nop 0\n"
    "s_cbranch_vccnz .Lh_slow1\n"
    ".Lh_ok1:\n"
    "ds_write_b128 v10, v[72:75] offset:8192\n"
    "ds_read_b128 v[44:47], v9 offset:4096\n"
    "s_waitcnt lgkmcnt(4)\n"
    "v_mfma_f32_16x16x32_bf16 v[52:55], v[164:167], v[32:35], v[52:55]\n"
    "ds_write_b128 v10, v[76:79] offset:9216\n"
    "s_add_u32 s65, s69, 0xc000\n"
    "v_mfma_f32_16x16x32_bf16 v[56:59], v[196:199], v[32:35], v[56:59]\n"
    "buffer_load_dwordx4 v[104:107], v1, s[36:39], s65 offen sc1\n"
    "buffer_load_dwordx4 v[108:111], v1, s[36:39], s65 offen offset:1024 sc1\n"
    "v_mfma_f32_16x16x32_bf16 v[60:63], v[228:231], v[32:35], v[60:63]\n"
    "s_waitcnt vmcnt(7)\n"
    "v_max3_u32 v11, v80, v81, v82\n"
    "s_waitcnt vmcnt(6)\n"
    "v_max3_u32 v11, v11, v83, v84\n"
    "ds_read_b128 v[48:51], v9 offset:5120\n"
    "s_waitcnt lgkmcnt(5)\n"
    "v_mfma_f32_16x16x32_bf16 v[52:55], v[168:171], v[36:39], v[52:55]\n"
    "v_max3_u32 v11, v11, v85, v86\n"
    "v_max_u32 v11, v11, v87\n"
    "v_mfma_f32_16x16x32_bf16 v[56:59], v[200:203], v[36:39], v[56:59]\n"
    "v_cmp_eq_u32 vcc, -1, v11\n"
    "s_nop 0\n"
    "s_cbranch_vccnz .Lh_slow2\n"
    ".Lh_ok2:\n"
    "ds_write_b128 v10, v[80:83] offset:24576\n"
    "v_mfma_f32_16x16x32_bf16 v[60:63], v[232:235], v[36:39], v[60:63]\n"
    "ds_write_b128 v10, v[84:87] offset:25600\n"
    "s_add_u32 s65, s69, 0xa000\n"
    "ds_read_b128 v[28:31], v9 offset:6144\n"
    "s_waitcnt lgkmcnt(7)\n"
    "v_mfma_f32_16x16x32_bf16 v[52:55], v[172:175], v[40:43], v[52:55]\n"
    "buffer_load_dwordx4 v[64:67], v1, s[36:39], s65 offen sc1\n"
    "buffer_load_dwordx4 v[68:71], v1, s[36:39], s65 offen offset:1024 sc1\n"
    "v_mfma_f32_16x16x32_bf16 v[56:59], v[204:207], v[40:43], v[56:59]\n"
    "s_waitcnt vmcnt(7)\n"
    "v_max3_u32 v11, v88, v89, v90\n"
    "s_waitcnt vmcnt(6)\n"
    "v_max3_u32 v11, v11, v91, v92\n"
    "v_mfma_f32_16x16x32_bf16 v[60:63], v[236:239], v[40:43], v[60:63]\n"
    "v_max3_u32 v11, v11, v93, v94\n"
    "v_max_u32 v11, v11, v95\n"
    "ds_read_b128 v[32:35], v9 offset:7168\n"
    "s_waitcnt lgkmcnt(6)\n"
    "v_mfma_f32_16x16x32_bf16 v[52:55], v[176:179], v[44:47], v[52:55]\n"
    "v_cmp_eq_u32 vcc, -1, v11\n"
    "s_nop 0\n"
    "s_cbranch_vccnz .Lh_slow3\n"
    ".Lh_ok3:\n"
    "ds_write_b128 v10, v[88:91] offset:16384\n"
    "v_mfma_f32_16x16x32_bf16 v[56:59], v[208:211], v[44:47], v[56:59]\n"
    "ds_write_b128 v10, v[92:95] offset:17408\n"
    "s_add_u32 s65, s69, 0x10000\n"
    "v_mfma_f32_16x16x32_bf16 v[60:63], v[240:243], v[44:47], v[60:63]\n"
    "buffer_load_dwordx4 v[72:75], v1, s[36:39], s65 offen sc1\n"
    "buffer_load_dwordx4 v[76:79], v1, s[36:39], s65 offen offset:1024 sc1\n"
    "s_waitcnt lgkmcnt(6)\n"
    "v_mfma_f32_16x16x32_bf16 v[52:55], v[180:183], v[48:51], v[52:55]\n"
    "s_add_u32 s64, s70, 0x3000\n"
    "s_add_u32 m0, s33, 0x17800\n"
    "v_mfma_f32_16x16x32_bf16 v[56:59], v[212:215], v[48:51], v[56:59]\n"
    "buffer_load_dwordx4 v1, s[36:39], s64 offen sc1 lds\n"
    "s_add_u32 s64, s70, 0x19000\n"
    "v_mfma_f32_16x16x32_bf16 v[60:63], v[244:247], v[48:51], v[60:63]\n"
    "s_add_u32 m0, s33, 0x20800\n"
    "s_nop 0\n"
    "buffer_load_dwordx4 v1, s[36:39], s64 offen sc1 lds\n"
    "s_waitcnt lgkmcnt(3)\n"
    "v_mfma_f32_16x16x32_bf16 v[52:55], v[184:187], v[28:31], v[52:55]\n"
    "v_mfma_f32_16x16x32_bf16 v[56:59], v[216:219], v[28:31], v[56:59]\n"
    "v_mfma_f32_16x16x32_bf16 v[60:63], v[248:251], v[28:31], v[60:63]\n"
    "s_waitcnt lgkmcnt(2)\n"
    "v_mfma_f32_16x16x32_bf16 v[52:55], v[188:191], v[32:35], v[52:55]\n"
    "v_mfma_f32_16x16x32_bf16 v[56:59], v[220:223], v[32:35], v[56:59]\n"
    "v_mfma_f32_16x16x32_bf16 v[60:63], v[252:255], v[32:35], v[60:63]\n"
    "s_nop 5\n"
    "ds_write_b128 v8, v[52:55]\n"
    "ds_write_b128 v8, v[56:59] offset:1024\n"
    "ds_write_b128 v8, v[60:63] offset:2048\n"
    "s_waitcnt lgkmcnt(0)\n"
    "s_barrier\n"
    "ds_read_b128 v[36:39], v9 offset:8192\n"
    "ds_read_b128 v[40:43], v9 offset:9216\n"
    "ds_read_b128 v[44:47], v9 offset:10240\n"
    "ds_read_b128 v[48:51], v9 offset:11264\n"
    "s_waitcnt lgkmcnt(3)\n"
    "v_mfma_f32_16x16x32_bf16 v[52:55], v[160:163], v[36:39], 0\n"
    "v_mfma_f32_16x16x32_bf16 v[56:59], v[192:195], v[36:39], 0\n"
    "v_mfma_f32_16x16x32_bf16 v[60:63], v[224:227], v[36:39], 0\n"
    "ds_read_b128 v[28:31], v9 offset:12288\n"
    "s_waitcnt lgkmcnt(3)\n"
    "v_mfma_f32_16x16x32_bf16 v[52:55], v[164:167], v[40:43], v[52:55]\n"
    "v_mfma_f32_16x16x32_bf16 v[56:59], v[196:199], v[40:43], v[56:59]\n"
    "v_mfma_f32_16x16x32_bf16 v[60:63], v[228:231], v[40:43], v[60:63]\n"
    "ds_read_b128 v[32:35], v9 offset:13312\n"
    "s_waitcnt lgkmcnt(3)\n"
    "v_mfma_f32_16x16x32_bf16 v[52:55], v[168:171], v[44:47], v[52:55]\n"
    "v_mfma_f32_16x16x32_bf16 v[56:59], v[200:203], v[44:47], v[56:59]\n"
    "v_mfma_f32_16x16x32_bf16 v[60:63], v[232:235], v[44:47], v[60:63]\n"
    "ds_read_b128 v[36:39], v9 offset:14336\n"
    "s_waitcnt lgkmcnt(3)\n"
    "v_mfma_f32_16x16x32_bf16 v[52:55], v[172:175], v[48:51], v[52:55]\n"
    "v_mfma_f32_16x16x32_bf16 v[56:59], v[204:207], v[48:51], v[56:59]\n"
    "v_mfma_f32_16x16x32_bf16 v[60:63], v[236:239], v[48:51], v[60:63]\n"
    "ds_read_b128 v[40:43], v9 offset:15360\n"
    "s_waitcnt lgkmcnt(3)\n"
    "v_mfma_f32_16x16x32_bf16 v[52:55], v[176:179], v[28:31], v[52:55]\n"
    "v_mfma_f32_16x16x32_bf16 v[56:59], v[208:211], v[28:31], v[56:59]\n"
    "v_mfma_f32_16x16x32_bf16 v[60:63], v[240:243], v[28:31], v[60:63]\n"
    "ds_read_b128 v[44:47], v9 offset:16384\n"
    "s_waitcnt lgkmcnt(3)\n"
    "v_mfma_f32_16x16x32_bf16 v[52:55], v[180:183], v[32:35], v[52:55]\n"
    "v_mfma_f32_16x16x32_bf16 v[56:59], v[212:215], v[32:35], v[56:59]\n"
    "v_mfma_f32_16x16x32_bf16 v[60:63], v[244:247], v[32:35], v[60:63]\n"
    "ds_read_b128 v[48:51], v9 offset:17408\n"
    "s_waitcnt lgkmcnt(3)\n"
    "v_mfma_f32_16x16x32_bf16 v[52:55], v[184:187], v[36:39], v[52:55]\n"
    "v_mfma_f32_16x16x32_bf16 v[56:59], v[216:219], v[36:39], v[56:59]\n"
    "v_mfma_f32_16x16x32_bf16 v[60:63], v[248:251], v[36:39], v[60:63]\n"
    "ds_read_b128 v[28:31], v9 offset:18432\n"
    "s_waitcnt lgkmcnt(3)\n"
    "v_mfma_f32_16x16x32_bf16 v[52:55], v[188:191], v[40:43], v[52:55]\n"
    "v_mfma_f32_16x16x32_bf16 v[56:59], v[220:223], v[40:43], v[56:59]\n"
    "v_mfma_f32_16x16x32_bf16 v[60:63], v[252:255], v[40:43], v[60:63]\n"
    "ds_read_b128 v[32:35], v9 offset:19456\n"
    "s_nop 4\n"
    "ds_write_b128 v8, v[52:55] offset:12288\n"
    "ds_write_b128 v8, v[56:59] offset:13312\n"
    "ds_write_b128 v8, v[60:63] offset:14336\n"
    "s_waitcnt vmcnt(9)\n"
    "v_max3_u32 v11, v96, v97, v98\n"
    "s_waitcnt vmcnt(8)\n"
    "v_max3_u32 v11, v11, v99, v100\n"
    "v_max3_u32 v11, v11, v101, v102\n"
    "v_max_u32 v11, v11, v103\n"
    "v_cmp_eq_u32 vcc, -1, v11\n"
    "s_nop 0\n"
    "s_cbranch_vccnz .Lh_slow4\n"
    ".Lh_ok4:\n"
    "ds_write_b128 v10, v[96:99] offset:32768\n"
    "ds_write_b128 v10, v[100:103] offset:33792\n"
    "s_add_u32 s65, s69, 0xe000\n"
    "buffer_load_dwordx4 v[80:83], v1, s[36:39], s65 offen sc1\n"
    "buffer_load_dwordx4 v[84:87], v1, s[36:39], s65 offen offset:1024 sc1\n"
    "s_waitcnt vmcnt(9)\n"
    "v_max3_u32 v11, v104, v105, v106\n"
    "s_waitcnt vmcnt(8)\n"
    "v_max3_u32 v11, v11, v107, v108\n"
    "v_max3_u32 v11, v11, v109, v110\n"
    "v_max_u32 v11, v11, v111\n"
    "v_cmp_eq_u32 vcc, -1, v11\n"
    "s_nop 0\n"
    "s_cbranch_vccnz .Lh_slow5\n"
    ".Lh_ok5:\n"
    "ds_write_b128 v10, v[104:107]\n"
    "ds_write_b128 v10, v[108:111] offset:1024\n"
    "s_add_u32 s65, s69, 0x14000\n"
    "buffer_load_dwordx4 v[88:91], v1, s[36:39], s65 offen sc1\n"
    "buffer_load_dwordx4 v[92:95], v1, s[36:39], s65 offen offset:1024 sc1\n"
    "s_waitcnt vmcnt(9)\n"
    "v_max3_u32 v11, v64, v65, v66\n"
    "s_waitcnt vmcnt(8)\n"
    "v_max3_u32 v11, v11, v67, v68\n"
    "v_max3_u32 v11, v11, v69, v70\n"
    "v_max_u32 v11, v11, v71\n"
    "v_cmp_eq_u32 vcc, -1, v11\n"
    "s_nop 0\n"
    "s_cbranch_vccnz .Lh_slow6\n"
    ".Lh_ok6:\n"
    "ds_write_b128 v10, v[64:67] offset:40960\n"
    "ds_write_b128 v10, v[68:71] offset:41984\n"
    "s_add_u32 s65, s69, 0x12000\n"
    "buffer_load_dwordx4 v[96:99], v1, s[36:39], s65 offen sc1\n"
    "buffer_load_dwordx4 v[100:103], v1, s[36:39], s65 offen offset:1024 sc1\n"
    "s_add_u32 s64, s70, 0x4000\n"
    "s_add_u32 m0, s33, 0x18800\n"
    "s_nop 0\n"
    "buffer_load_dwordx4 v1, s[36:39], s64 offen sc1 lds\n"
    "s_add_u32 s64, s70, 0x1a000\n"
    "s_add_u32 m0, s33, 0x21800\n"
    "s_nop 0\n"
    "buffer_load_dwordx4 v1, s[36:39], s64 offen sc1 lds\n"
    "s_waitcnt lgkmcnt(0)\n"
    "s_barrier\n"
    "v_mfma_f32_16x16x32_bf16 v[52:55], v[160:163], v[44:47], 0\n"
    "v_mfma_f32_16x16x32_bf16 v[56:59], v[192:195], v[44:47], 0\n"
    "v_mfma_f32_16x16x32_bf16 v[60:63], v[224:227], v[44:47], 0\n"
    "ds_read_b128 v[36:39], v9 offset:20480\n"
    "v_mfma_f32_16x16x32_bf16 v[52:55], v[164:167], v[48:51], v[52:55]\n"
    "v_mfma_f32_16x16x32_bf16 v[56:59], v[196:199], v[48:51], v[56:59]\n"
    "v_mfma_f32_16x16x32_bf16 v[60:63], v[228:231], v[48:51], v[60:63]\n"
    "ds_read_b128 v[40:43], v9 offset:21504\n"
    "v_mfma_f32_16x16x32_bf16 v[52:55], v[168:171], v[28:31], v[52:55]\n"
    "v_mfma_f32_16x16x32_bf16 v[56:59], v[200:203], v[28:31], v[56:59]\n"
    "v_mfma_f32_16x16x32_bf16 v[60:63], v[232:235], v[28:31], v[60:63]\n"
    "ds_read_b128 v[44:47], v9 offset:22528\n"
    "v_mfma_f32_16x16x32_bf16 v[52:55], v[172:175], v[32:35], v[52:55]\n"
    "v_mfma_f32_16x16x32_bf16 v[56:59], v[204:207], v[32:35], v[56:59]\n"
    "v_mfma_f32_16x16x32_bf16 v[60:63], v[236:239], v[32:35], v[60:63]\n"
    "ds_read_b128 v[48:51], v9 offset:23552\n"
    "s_waitcnt lgkmcnt(3)\n"
    "v_mfma_f32_16x16x32_bf16 v[52:55], v[176:179], v[36:39], v[52:55]\n"
    "v_mfma_f32_16x16x32_bf16 v[56:59], v[208:211], v[36:39], v[56:59]\n"
    "v_mfma_f32_16x16x32_bf16 v[60:63], v[240:243], v[36:39], v[60:63]\n"
    "ds_read_b128 v[28:31], v9\n"
    "s_waitcnt lgkmcnt(3)\n"
    "v_mfma_f32_16x16x32_bf16 v[52:55], v[180:183], v[40:43], v[52:55]\n"
    "v_mfma_f32_16x16x32_bf16 v[56:59], v[212:215], v[40:43], v[56:59]\n"
    "v_mfma_f32_16x16x32_bf16 v[60:63], v[244:247], v[40:43], v[60:63]\n"
    "ds_read_b128 v[32:35], v9 offset:1024\n"
    "s_waitcnt lgkmcnt(3)\n"
    "v_mfma_f32_16x16x32_bf16 v[52:55], v[184:187], v[44:47], v[52:55]\n"
    "v_mfma_f32_16x16x32_bf16 v[56:59], v[216:219], v[44:47], v[56:59]\n"
    "v_mfma_f32_16x16x32_bf16 v[60:63], v[248:251], v[44:47], v[60:63]\n"
    "ds_read_b128 v[36:39], v9 offset:2048\n"
    "s_waitcnt lgkmcnt(3)\n"
    "v_mfma_f32_16x16x32_bf16 v[52:55], v[188:191], v[48:51], v[52:55]\n"
    "v_mfma_f32_16x16x32_bf16 v[56:59], v[220:223], v[48:51], v[56:59]\n"
    "v_mfma_f32_16x16x32_bf16 v[60:63], v[252:255], v[48:51], v[60:63]\n"
    "ds_read_b128 v[40:43], v9 offset:3072\n"
    "s_nop 4\n"
    "ds_write_b128 v8, v[52:55]\n"
    "ds_write_b128 v8, v[56:59] offset:1024\n"
    "ds_write_b128 v8, v[60:63] offset:2048\n"
    "s_waitcnt vmcnt(11)\n"
    "v_max3_u32 v11, v72, v73, v74\n"
    "s_waitcnt vmcnt(10)\n"
    "v_max3_u32 v11, v11, v75, v76\n"
    "v_max3_u32 v11, v11, v77, v78\n"
    "v_max_u32 v11, v11, v79\n"
    "v_cmp_eq_u32 vcc, -1, v11\n"
    "s_nop 0\n"
    "s_cbranch_vccnz .Lh_slow7\n"
    ".Lh_ok7:\n"
    "ds_write_b128 v10, v[72:75] offset:8192\n"
    "ds_write_b128 v10, v[76:79] offset:9216\n"
    "s_add_u32 s65, s69, 0x18000\n"
    "buffer_load_dwordx4 v[104:107], v1, s[36:39], s65 offen sc1\n"
    "buffer_load_dwordx4 v[108:111], v1, s[36:39], s65 offen offset:1024 sc1\n"
    "s_waitcnt vmcnt(9)\n"
    "v_max3_u32 v11, v80, v81, v82\n"
    "s_waitcnt vmcnt(8)\n"
    "v_max3_u32 v11, v11, v83, v84\n"
    "v_max3_u32 v11, v11, v85, v86\n"
    "v_max_u32 v11, v11, v87\n"
    "v_cmp_eq_u32 vcc, -1, v11\n"
    "s_nop 0\n"
    "s_cbranch_vccnz .Lh_slow8\n"
    ".Lh_ok8:\n"
    "ds_write_b128 v10, v[80:83] offset:49152\n"
    "ds_write_b128 v10, v[84:87] offset:50176\n"
    "s_add_u32 s65, s69, 0x16000\n"
    "buffer_load_dwordx4 v[64:67], v1, s[36:39], s65 offen sc1\n"
    "buffer_load_dwordx4 v[68:71], v1, s[36:39], s65 offen offset:1024 sc1\n"
    "s_add_u32 s64, s70, 0x5000\n"
    "s_add_u32 m0, s33, 0x19800\n"
    "s_nop 0\n"
    "buffer_load_dwordx4 v1, s[36:39], s64 offen sc1 lds\n"
    "s_add_u32 s64, s70, 0x1b000\n"
    "s_add_u32 m0, s33, 0x22800\n"
    "s_nop 0\n"
    "buffer_load_dwordx4 v1, s[36:39], s64 offen sc1 lds\n"
    "s_waitcnt lgkmcnt(0)\n"
    "s_barrier\n"
    "v_mfma_f32_16x16x32_bf16 v[52:55], v[160:163], v[28:31], 0\n"
    "v_mfma_f32_16x16x32_bf16 v[56:59], v[192:195], v[28:31], 0\n"
    "v_mfma_f32_16x16x32_bf16 v[60:63], v[224:227], v[28:31], 0\n"
    "ds_read_b128 v[44:47], v9 offset:4096\n"
    "v_mfma_f32_16x16x32_bf16 v[52:55], v[164:167], v[32:35], v[52:55]\n"
    "v_mfma_f32_16x16x32_bf16 v[56:59], v[196:199], v[32:35], v[56:59]\n"
    "v_mfma_f32_16x16x32_bf16 v[60:63], v[228:231], v[32:35], v[60:63]\n"
    "ds_read_b128 v[48:51], v9 offset:5120\n"
    "v_mfma_f32_16x16x32_bf16 v[52:55], v[168:171], v[36:39], v[52:55]\n"
    "v_mfma_f32_16x16x32_bf16 v[56:59], v[200:203], v[36:39], v[56:59]\n"
    "v_mfma_f32_16x16x32_bf16 v[60:63], v[232:235], v[36:39], v[60:63]\n"
    "ds_read_b128 v[28:31], v9 offset:6144\n"
    "v_mfma_f32_16x16x32_bf16 v[52:55], v[172:175], v[40:43], v[52:55]\n"
    "v_mfma_f32_16x16x32_bf16 v[56:59], v[204:207], v[40:43], v[56:59]\n"
    "v_mfma_f32_16x16x32_bf16 v[60:63], v[236:239], v[40:43], v[60:63]\n"
    "ds_read_b128 v[32:35], v9 offset:7168\n"
    "s_waitcnt lgkmcnt(3)\n"
    "v_mfma_f32_16x16x32_bf16 v[52:55], v[176:179], v[44:47], v[52:55]\n"
    "v_mfma_f32_16x16x32_bf16 v[56:59], v[208:211], v[44:47], v[56:59]\n"
    "v_mfma_f32_16x16x32_bf16 v[60:63], v[240:243], v[44:47], v[60:63]\n"
    "ds_read_b128 v[36:39], v9 offset:8192\n"
    "s_waitcnt lgkmcnt(3)\n"
    "v_mfma_f32_16x16x32_bf16 v[52:55], v[180:183], v[48:51], v[52:55]\n"
    "v_mfma_f32_16x16x32_bf16 v[56:59], v[212:215], v[48:51], v[56:59]\n"
    "v_mfma_f32_16x16x32_bf16 v[60:63], v[244:247], v[48:51], v[60:63]\n"
    "ds_read_b128 v[40:43], v9 offset:9216\n"
    "s_waitcnt lgkmcnt(3)\n"
    "v_mfma_f32_16x16x32_bf16 v[52:55], v[184:187], v[28:31], v[52:55]\n"
    "v_mfma_f32_16x16x32_bf16 v[56:59], v[216:219], v[28:31], v[56:59]\n"
    "v_mfma_f32_16x16x32_bf16 v[60:63], v[248:251], v[28:31], v[60:63]\n"
    "ds_read_b128 v[44:47], v9 offset:10240\n"
    "s_waitcnt lgkmcnt(3)\n"
    "v_mfma_f32_16x16x32_bf16 v[52:55], v[188:191], v[32:35], v[52:55]\n"
    "v_mfma_f32_16x16x32_bf16 v[56:59], v[220:223], v[32:35], v[56:59]\n"
    "v_mfma_f32_16x16x32_bf16 v[60:63], v[252:255], v[32:35], v[60:63]\n"
    "ds_read_b128 v[48:51], v9 offset:11264\n"
    "s_nop 4\n"
    "ds_write_b128 v8, v[52:55] offset:12288\n"
    "ds_write_b128 v8, v[56:59] offset:13312\n"
    "ds_write_b128 v8, v[60:63] offset:14336\n"
    "s_waitcnt vmcnt(11)\n"
    "v_max3_u32 v11, v88, v89, v90\n"
    "s_waitcnt vmcnt(10)\n"
    "v_max3_u32 v11, v11, v91, v92\n"
    "v_max3_u32 v11, v11, v93, v94\n"
    "v_max_u32 v11, v11, v95\n"
    "v_cmp_eq_u32 vcc, -1, v11\n"
    "s_nop 0\n"
    "s_cbranch_vccnz .Lh_slow9\n"
    ".Lh_ok9:\n"
    "ds_write_b128 v10, v[88:91] offset:16384\n"
    "ds_write_b128 v10, v[92:95] offset:17408\n"
    "s_add_u32 s65, s69, 0x1c000\n"
    "buffer_load_dwordx4 v[72:75], v1, s[36:39], s65 offen sc1\n"
    "buffer_load_dwordx4 v[76:79], v1, s[36:39], s65 offen offset:1024 sc1\n"
    "s_waitcnt vmcnt(11)\n"
    "v_max3_u32 v11, v96, v97, v98\n"
    "s_waitcnt vmcnt(10)\n"
    "v_max3_u32 v11, v11, v99, v100\n"
    "v_max3_u32 v11, v11, v101, v102\n"
    "v_max_u32 v11, v11, v103\n"
    "v_cmp_eq_u32 vcc, -1, v11\n"
    "s_nop 0\n"
    "s_cbranch_vccnz .Lh_slow10\n"
    ".Lh_ok10:\n"
    "ds_write_b128 v10, v[96:99] offset:24576\n"
    "ds_write_b128 v10, v[100:103] offset:25600\n"
    "s_add_u32 s65, s69, 0x1a000\n"
    "buffer_load_dwordx4 v[80:83], v1, s[36:39], s65 offen sc1\n"
    "buffer_load_dwordx4 v[84:87], v1, s[36:39], s65 offen offset:1024 sc1\n"
    "s_add_u32 s64, s70, 0x6000\n"
    "s_add_u32 m0, s33, 0x1a800\n"
    "s_nop 0\n"
    "buffer_load_dwordx4 v1, s[36:39], s64 offen sc1 lds\n"
    "s_add_u32 s64, s70, 0x1c000\n"
    "s_add_u32 m0, s33, 0x23800\n"
    "s_nop 0\n"
    "buffer_load_dwordx4 v1, s[36:39], s64 offen sc1 lds\n"
    "s_waitcnt lgkmcnt(0)\n"
    "s_barrier\n"
    "v_mfma_f32_16x16x32_bf16 v[52:55], v[160:163], v[36:39], 0\n"
    "v_mfma_f32_16x16x32_bf16 v[56:59], v[192:195], v[36:39], 0\n"
    "v_mfma_f32_16x16x32_bf16 v[60:63], v[224:227], v[36:39], 0\n"
    "ds_read_b128 v[28:31], v9 offset:12288\n"
    "v_mfma_f32_16x16x32_bf16 v[52:55], v[164:167], v[40:43], v[52:55]\n"
    "v_mfma_f32_16x16x32_bf16 v[56:59], v[196:199], v[40:43], v[56:59]\n"
    "v_mfma_f32_16x16x32_bf16 v[60:63], v[228:231], v[40:43], v[60:63]\n"
    "ds_read_b128 v[32:35], v9 offset:13312\n"
    "v_mfma_f32_16x16x32_bf16 v[52:55], v[168:171], v[44:47], v[52:55]\n"
    "v_mfma_f32_16x16x32_bf16 v[56:59], v[200:203], v[44:47], v[56:59]\n"
    "v_mfma_f32_16x16x32_bf16 v[60:63], v[232:235], v[44:47], v[60:63]\n"
    "ds_read_b128 v[36:39], v9 offset:14336\n"
    "v_mfma_f32_16x16x32_bf16 v[52:55], v[172:175], v[48:51], v[52:55]\n"
    "v_mfma_f32_16x16x32_bf16 v[56:59], v[204:207], v[48:51], v[56:59]\n"
    "v_mfma_f32_16x16x32_bf16 v[60:63], v[236:239], v[48:51], v[60:63]\n"
    "ds_read_b128 v[40:43], v9 offset:15360\n"
    "s_waitcnt lgkmcnt(3)\n"
    "v_mfma_f32_16x16x32_bf16 v[52:55], v[176:179], v[28:31], v[52:55]\n"
    "v_mfma_f32_16x16x32_bf16 v[56:59], v[208:211], v[28:31], v[56:59]\n"
    "v_mfma_f32_16x16x32_bf16 v[60:63], v[240:243], v[28:31], v[60:63]\n"
    "ds_read_b128 v[44:47], v9 offset:16384\n"
    "s_waitcnt lgkmcnt(3)\n"
    "v_mfma_f32_16x16x32_bf16 v[52:55], v[180:183], v[32:35], v[52:55]\n"
    "v_mfma_f32_16x16x32_bf16 v[56:59], v[212:215], v[32:35], v[56:59]\n"
    "v_mfma_f32_16x16x32_bf16 v[60:63], v[244:247], v[32:35], v[60:63]\n"
    "ds_read_b128 v[48:51], v9 offset:17408\n"
    "s_waitcnt lgkmcnt(3)\n"
    "v_mfma_f32_16x16x32_bf16 v[52:55], v[184:187], v[36:39], v[52:55]\n"
    "v_mfma_f32_16x16x32_bf16 v[56:59], v[216:219], v[36:39], v[56:59]\n"
    "v_mfma_f32_16x16x32_bf16 v[60:63], v[248:251], v[36:39], v[60:63]\n"
    "ds_read_b128 v[28:31], v9 offset:18432\n"
    "s_waitcnt lgkmcnt(3)\n"
    "v_mfma_f32_16x16x32_bf16 v[52:55], v[188:191], v[40:43], v[52:55]\n"
    "v_mfma_f32_16x16x32_bf16 v[56:59], v[220:223], v[40:43], v[56:59]\n"
    "v_mfma_f32_16x16x32_bf16 v[60:63], v[252:255], v[40:43], v[60:63]\n"
    "ds_read_b128 v[32:35], v9 offset:19456\n"
    "s_nop 4\n"
    "ds_write_b128 v8, v[52:55]\n"
    "ds_write_b128 v8, v[56:59] offset:1024\n"
    "ds_write_b128 v8, v[60:63] offset:2048\n"
    "s_waitcnt vmcnt(11)\n"
    "v_max3_u32 v11, v104, v105, v106\n"
    "s_waitcnt vmcnt(10)\n"
    "v_max3_u32 v11, v11, v107, v108\n"
    "v_max3_u32 v11, v11, v109, v110\n"
    "v_max_u32 v11, v11, v111\n"
    "v_cmp_eq_u32 vcc, -1, v11\n"
    "s_nop 0\n"
    "s_cbranch_vccnz .Lh_slow11\n"
    ".Lh_ok11:\n"
    "ds_write_b128 v10, v[104:107]\n"
    "ds_write_b128 v10, v[108:111] offset:1024\n"
    "s_add_u32 s65, s69, 0x1e000\n"
    "buffer_load_dwordx4 v[88:91], v1, s[36:39], s65 offen sc1\n"
    "buffer_load_dwordx4 v[92:95], v1, s[36:39], s65 offen offset:1024 sc1\n"
    "s_waitcnt vmcnt(11)\n"
    "v_max3_u32 v11, v64, v65, v66\n"
    "s_waitcnt vmcnt(10)\n"
    "v_max3_u32 v11, v11, v67, v68\n"
    "v_max3_u32 v11, v11, v69, v70\n"
    "v_max_u32 v11, v11, v71\n"
    "v_cmp_eq_u32 vcc, -1, v11\n"
    "s_nop 0\n"
    "s_cbranch_vccnz .Lh_slow12\n"
    ".Lh_ok12:\n"
    "ds_write_b128 v10, v[64:67] offset:32768\n"
    "ds_write_b128 v10, v[68:71] offset:33792\n"
    "s_add_u32 s64, s70, 0x7000\n"
    "s_add_u32 m0, s33, 0x1b800\n"
    "s_nop 0\n"
    "buffer_load_dwordx4 v1, s[36:39], s64 offen sc1 lds\n"
    "s_add_u32 s64, s70, 0x1d000\n"
    "s_add_u32 m0, s33, 0x24800\n"
    "s_nop 0\n"
    "buffer_load_dwordx4 v1, s[36:39], s64 offen sc1 lds\n"
    "s_waitcnt lgkmcnt(0)\n"
    "s_barrier\n"
    "v_mfma_f32_16x16x32_bf16 v[52:55], v[160:163], v[44:47], 0\n"
    "v_mfma_f32_16x16x32_bf16 v[56:59], v[192:195], v[44:47], 0\n"
    "v_mfma_f32_16x16x32_bf16 v[60:63], v[224:227], v[44:47], 0\n"
    "ds_read_b128 v[36:39], v9 offset:20480\n"
    "v_mfma_f32_16x16x32_bf16 v[52:55], v[164:167], v[48:51], v[52:55]\n"
    "v_mfma_f32_16x16x32_bf16 v[56:59], v[196:199], v[48:51], v[56:59]\n"
    "v_mfma_f32_16x16x32_bf16 v[60:63], v[228:231], v[48:51], v[60:63]\n"
    "ds_read_b128 v[40:43], v9 offset:21504\n"
    "v_mfma_f32_16x16x32_bf16 v[52:55], v[168:171], v[28:31], v[52:55]\n"
    "v_mfma_f32_16x16x32_bf16 v[56:59], v[200:203], v[28:31], v[56:59]\n"
    "v_mfma_f32_16x16x32_bf16 v[60:63], v[232:235], v[28:31], v[60:63]\n"
    "ds_read_b128 v[44:47], v9 offset:22528\n"
    "v_mfma_f32_16x16x32_bf16 v[52:55], v[172:175], v[32:35], v[52:55]\n"
    "v_mfma_f32_16x16x32_bf16 v[56:59], v[204:207], v[32:35], v[56:59]\n"
    "v_mfma_f32_16x16x32_bf16 v[60:63], v[236:239], v[32:35], v[60:63]\n"
    "ds_read_b128 v[48:51], v9 offset:23552\n"
    "s_waitcnt lgkmcnt(3)\n"
    "v_mfma_f32_16x16x32_bf16 v[52:55], v[176:179], v[36:39], v[52:55]\n"
    "v_mfma_f32_16x16x32_bf16 v[56:59], v[208:211], v[36:39], v[56:59]\n"
    "v_mfma_f32_16x16x32_bf16 v[60:63], v[240:243], v[36:39], v[60:63]\n"
    "ds_read_b128 v[28:31], v9\n"
    "s_waitcnt lgkmcnt(3)\n"
    "v_mfma_f32_16x16x32_bf16 v[52:55], v[180:183], v[40:43], v[52:55]\n"
    "v_mfma_f32_16x16x32_bf16 v[56:59], v[212:215], v[40:43], v[56:59]\n"
    "v_mfma_f32_16x16x32_bf16 v[60:63], v[244:247], v[40:43], v[60:63]\n"
    "ds_read_b128 v[32:35], v9 offset:1024\n"
    "s_waitcnt lgkmcnt(3)\n"
    "v_mfma_f32_16x16x32_bf16 v[52:55], v[184:187], v[44:47], v[52:55]\n"
    "v_mfma_f32_16x16x32_bf16 v[56:59], v[216:219], v[44:47], v[56:59]\n"
    "v_mfma_f32_16x16x32_bf16 v[60:63], v[248:251], v[44:47], v[60:63]\n"
    "ds_read_b128 v[36:39], v9 offset:2048\n"
    "s_waitcnt lgkmcnt(3)\n"
    "v_mfma_f32_16x16x32_bf16 v[52:55], v[188:191], v[48:51], v[52:55]\n"
    "v_mfma_f32_16x16x32_bf16 v[56:59], v[220:223], v[48:51], v[56:59]\n"
    "v_mfma_f32_16x16x32_bf16 v[60:63], v[252:255], v[48:51], v[60:63]\n"
    "ds_read_b128 v[40:43], v9 offset:3072\n"
    "s_nop 4\n"
    "ds_write_b128 v8, v[52:55] offset:12288\n"
    "ds_write_b128 v8, v[56:59] offset:13312\n"
    "ds_write_b128 v8, v[60:63] offset:14336\n"
    "s_waitcnt vmcnt(9)\n"
    "v_max3_u32 v11, v72, v73, v74\n"
    "s_waitcnt vmcnt(8)\n"
    "v_max3_u32 v11, v11, v75, v76\n"
    "v_max3_u32 v11, v11, v77, v78\n"
    "v_max_u32 v11, v11, v79\n"
    "v_cmp_eq_u32 vcc, -1, v11\n"
    "s_nop 0\n"
    "s_cbranch_vccnz .Lh_slow13\n"
    ".Lh_ok13:\n"
    "ds_write_b128 v10, v[72:75] offset:8192\n"
    "ds_write_b128 v10, v[76:79] offset:9216\n"
    "s_waitcnt vmcnt(7)\n"
    "v_max3_u32 v11, v80, v81, v82\n"
    "s_waitcnt vmcnt(6)\n"
    "v_max3_u32 v11, v11, v83, v84\n"
    "v_max3_u32 v11, v11, v85, v86\n"
    "v_max_u32 v11, v11, v87\n"
    "v_cmp_eq_u32 vcc, -1, v11\n"
    "s_nop 0\n"
    "s_cbranch_vccnz .Lh_slow14\n"
    ".Lh_ok14:\n"
    "ds_write_b128 v10, v[80:83] offset:40960\n"
    "ds_write_b128 v10, v[84:87] offset:41984\n"
    "s_add_u32 s64, s70, 0x8000\n"
    "s_add_u32 m0, s33, 0x1c800\n"
    "s_nop 0\n"
    "buffer_load_dwordx4 v1, s[36:39], s64 offen sc1 lds\n"
    "s_add_u32 s64, s70, 0x1e000\n"
    "s_add_u32 m0, s33, 0x25800\n"
    "s_nop 0\n"
    "buffer_load_dwordx4 v1, s[36:39], s64 offen sc1 lds\n"
    "s_waitcnt lgkmcnt(0)\n"
    "s_barrier\n"
    "v_mfma_f32_16x16x32_bf16 v[52:55], v[160:163], v[28:31], 0\n"
    "v_mfma_f32_16x16x32_bf16 v[56:59], v[192:195], v[28:31], 0\n"
    "v_mfma_f32_16x16x32_bf16 v[60:63], v[224:227], v[28:31], 0\n"
    "ds_read_b128 v[44:47], v9 offset:4096\n"
    "v_mfma_f32_16x16x32_bf16 v[52:55], v[164:167], v[32:35], v[52:55]\n"
    "v_mfma_f32_16x16x32_bf16 v[56:59], v[196:199], v[32:35], v[56:59]\n"
    "v_mfma_f32_16x16x32_bf16 v[60:63], v[228:231], v[32:35], v[60:63]\n"
    "ds_read_b128 v[48:51], v9 offset:5120\n"
    "v_mfma_f32_16x16x32_bf16 v[52:55], v[168:171], v[36:39], v[52:55]\n"
    "v_mfma_f32_16x16x32_bf16 v[56:59], v[200:203], v[36:39], v[56:59]\n"
    "v_mfma_f32_16x16x32_bf16 v[60:63], v[232:235], v[36:39], v[60:63]\n"
    "ds_read_b128 v[28:31], v9 offset:6144\n"
    "v_mfma_f32_16x16x32_bf16 v[52:55], v[172:175], v[40:43], v[52:55]\n"
    "v_mfma_f32_16x16x32_bf16 v[56:59], v[204:207], v[40:43], v[56:59]\n"
    "v_mfma_f32_16x16x32_bf16 v[60:63], v[236:239], v[40:43], v[60:63]\n"
    "ds_read_b128 v[32:35], v9 offset:7168\n"
    "s_waitcnt lgkmcnt(3)\n"
    "v_mfma_f32_16x16x32_bf16 v[52:55], v[176:179], v[44:47], v[52:55]\n"
    "v_mfma_f32_16x16x32_bf16 v[56:59], v[208:211], v[44:47], v[56:59]\n"
    "v_mfma_f32_16x16x32_bf16 v[60:63], v[240:243], v[44:47], v[60:63]\n"
    "ds_read_b128 v[36:39], v9 offset:8192\n"
    "s_waitcnt lgkmcnt(3)\n"
    "v_mfma_f32_16x16x32_bf16 v[52:55], v[180:183], v[48:51], v[52:55]\n"
    "v_mfma_f32_16x16x32_bf16 v[56:59], v[212:215], v[48:51], v[56:59]\n"
    "v_mfma_f32_16x16x32_bf16 v[60:63], v[244:247], v[48:51], v[60:63]\n"
    "ds_read_b128 v[40:43], v9 offset:9216\n"
    "s_waitcnt lgkmcnt(3)\n"
    "v_mfma_f32_16x16x32_bf16 v[52:55], v[184:187], v[28:31], v[52:55]\n"
    "v_mfma_f32_16x16x32_bf16 v[56:59], v[216:219], v[28:31], v[56:59]\n"
    "v_mfma_f32_16x16x32_bf16 v[60:63], v[248:251], v[28:31], v[60:63]\n"
    "ds_read_b128 v[44:47], v9 offset:10240\n"
    "s_waitcnt lgkmcnt(3)\n"
    "v_mfma_f32_16x16x32_bf16 v[52:55], v[188:191], v[32:35], v[52:55]\n"
    "v_mfma_f32_16x16x32_bf16 v[56:59], v[220:223], v[32:35], v[56:59]\n"
    "v_mfma_f32_16x16x32_bf16 v[60:63], v[252:255], v[32:35], v[60:63]\n"
    "ds_read_b128 v[48:51], v9 offset:11264\n"
    "s_nop 4\n"
    "ds_write_b128 v8, v[52:55]\n"
    "ds_write_b128 v8, v[56:59] offset:1024\n"
    "ds_write_b128 v8, v[60:63] offset:2048\n"
    "s_waitcnt vmcnt(5)\n"
    "v_max3_u32 v11, v88, v89, v90\n"
    "s_waitcnt vmcnt(4)\n"
    "v_max3_u32 v11, v11, v91, v92\n"
    "v_max3_u32 v11, v11, v93, v94\n"
    "v_max_u32 v11, v11, v95\n"
    "v_cmp_eq_u32 vcc, -1, v11\n"
    "s_nop 0\n"
    "s_cbranch_vccnz .Lh_slow15\n"
    ".Lh_ok15:\n"
    "ds_write_b128 v10, v[88:91] offset:49152\n"
    "ds_write_b128 v10, v[92:95] offset:50176\n"
    "s_waitcnt lgkmcnt(0)\n"
    "s_barrier\n"
    "v_mfma_f32_16x16x32_bf16 v[52:55], v[160:163], v[36:39], 0\n"
    "v_mfma_f32_16x16x32_bf16 v[56:59], v[192:195], v[36:39], 0\n"
    "v_mfma_f32_16x16x32_bf16 v[60:63], v[224:227], v[36:39], 0\n"
    "ds_read_b128 v[28:31], v9 offset:12288\n"
    "v_mfma_f32_16x16x32_bf16 v[52:55], v[164:167], v[40:43], v[52:55]\n"
    "v_mfma_f32_16x16x32_bf16 v[56:59], v[196:199], v[40:43], v[56:59]\n"
    "v_mfma_f32_16x16x32_bf16 v[60:63], v[228:231], v[40:43], v[60:63]\n"
    "ds_read_b128 v[32:35], v9 offset:13312\n"
    "v_mfma_f32_16x16x32_bf16 v[52:55], v[168:171], v[44:47], v[52:55]\n"
    "v_mfma_f32_16x16x32_bf16 v[56:59], v[200:203], v[44:47], v[56:59]\n"
    "v_mfma_f32_16x16x32_bf16 v[60:63], v[232:235], v[44:47], v[60:63]\n"
    "ds_read_b128 v[36:39], v9 offset:14336\n"
    "v_mfma_f32_16x16x32_bf16 v[52:55], v[172:175], v[48:51], v[52:55]\n"
    "v_mfma_f32_16x16x32_bf16 v[56:59], v[204:207], v[48:51], v[56:59]\n"
    "v_mfma_f32_16x16x32_bf16 v[60:63], v[236:239], v[48:51], v[60:63]\n"
    "ds_read_b128 v[40:43], v9 offset:15360\n"
    "s_waitcnt lgkmcnt(3)\n"
    "v_mfma_f32_16x16x32_bf16 v[52:55], v[176:179], v[28:31], v[52:55]\n"
    "v_mfma_f32_16x16x32_bf16 v[56:59], v[208:211], v[28:31], v[56:59]\n"
    "v_mfma_f32_16x16x32_bf16 v[60:63], v[240:243], v[28:31], v[60:63]\n"
    "s_waitcnt lgkmcnt(2)\n"
    "v_mfma_f32_16x16x32_bf16 v[52:55], v[180:183], v[32:35], v[52:55]\n"
    "v_mfma_f32_16x16x32_bf16 v[56:59], v[212:215], v[32:35], v[56:59]\n"
    "v_mfma_f32_16x16x32_bf16 v[60:63], v[244:247], v[32:35], v[60:63]\n"
    "s_waitcnt lgkmcnt(1)\n"
    "v_mfma_f32_16x16x32_bf16 v[52:55], v[184:187], v[36:39], v[52:55]\n"
    "v_mfma_f32_16x16x32_bf16 v[56:59], v[216:219], v[36:39], v[56:59]\n"
    "v_mfma_f32_16x16x32_bf16 v[60:63], v[248:251], v[36:39], v[60:63]\n"
    "s_waitcnt lgkmcnt(0)\n"
    "v_mfma_f32_16x16x32_bf16 v[52:55], v[188:191], v[40:43], v[52:55]\n"
    "v_mfma_f32_16x16x32_bf16 v[56:59], v[220:223], v[40:43], v[56:59]\n"
    "v_mfma_f32_16x16x32_bf16 v[60:63], v[252:255], v[40:43], v[60:63]\n"
    "s_nop 5\n"
    "ds_write_b128 v8, v[52:55] offset:12288\n"
    "ds_write_b128 v8, v[56:59] offset:13312\n"
    "ds_write_b128 v8, v[60:63] offset:14336\n"
    "s_waitcnt lgkmcnt(0)\n"
    "s_barrier\n"
    "s_waitcnt vmcnt(0)\n"
    "s_setprio 0\n"
    "s_barrier\n"
    "s_branch .Lp3_join\n"
    ".Lp3_fin:\n"
    "s_lshl_b32 s65, s52, 3\n"
    "s_lshl_b32 s60, s53, 1\n"
    "s_add_u32 s65, s65, s60\n"
    "s_lshl_b32 s65, s65, 11\n"
    "s_add_u32 s65, s65, 0xde8000\n"
    "s_add_u32 s64, s71, 0\n"
    "s_add_u32 m0, s35, 0x14800\n"
    "s_nop 0\n"
    "buffer_load_dwordx4 v1, s[36:39], s64 offen sc1 lds\n"
    "s_add_u32 s64, s71, 0x1000\n"
    "s_add_u32 m0, s35, 0x15800\n"
    "s_nop 0\n"
    "buffer_load_dwordx4 v1, s[36:39], s64 offen sc1 lds\n"
    "s_add_u32 s64, s71, 0x2000\n"
    "s_add_u32 m0, s35, 0x16800\n"
    "s_nop 0\n"
    "buffer_load_dwordx4 v1, s[36:39], s64 offen sc1 lds\n"
    "s_waitcnt vmcnt(3)\n"
    "buffer_load_dwordx4 v[224:227], v1, s[36:39], s68 offen sc1\n"
    "buffer_load_dwordx4 v[228:231], v1, s[36:39], s68 offen offset:1024 sc1\n"
    "buffer_load_dwordx4 v[232:235], v1, s[36:39], s68 offen offset:2048 sc1\n"
    "buffer_load_dwordx4 v[236:239], v1, s[36:39], s68 offen offset:3072 sc1\n"
    "s_add_u32 s68, s68, 0x1000\n"
    "buffer_load_dwordx4 v[240:243], v1, s[36:39], s68 offen sc1\n"
    "buffer_load_dwordx4 v[244:247], v1, s[36:39], s68 offen offset:1024 sc1\n"
    "buffer_load_dwordx4 v[248:251], v1, s[36:39], s68 offen offset:2048 sc1\n"
    "buffer_load_dwordx4 v[252:255], v1, s[36:39], s68 offen offset:3072 sc1\n"
    "buffer_load_dwordx4 v[92:95], v1, s[36:39], s65 offen sc1\n"
    "buffer_load_dwordx4 v[96:99], v1, s[36:39], s65 offen offset:1024 sc1\n"
    "buffer_load_dwordx4 v[100:103], v1, s[36:39], s65 offen offset:2048 sc1\n"
    "buffer_load_dwordx4 v[104:107], v1, s[36:39], s65 offen offset:3072 sc1\n"
    "v_lshlrev_b32 v10, 8, v2\n"
    "v_lshl_add_u32 v10, v3, 4, v10\n"
    "v_lshlrev_b32 v11, 4, v3\n"
    "v_add_u32 v11, 0x6000, v11\n"
    "v_mov_b32 v76, 0x4000000\n"
    "v_cmp_gt_u32 vcc, s55, v2\n"
    "s_nop 1\n"
    "v_cndmask_b32 v10, v76, v10, vcc\n"
    "s_mul_i32 s69, s52, 0x560\n"
    "s_add_u32 s69, s69, s54\n"
    "s_lshl_b32 s69, s69, 8\n"
    "s_add_u32 s69, s69, 0x888000\n"
    "s_barrier\n"
    "ds_read_b128 v[52:55], v8 offset:12288\n"
    "ds_read_b128 v[56:59], v8 offset:13312\n"
    "ds_read_b128 v[60:63], v8 offset:14336\n"
    "v_mfma_f32_16x16x32_bf16 v[12:15], v[160:163], v[24:27], 0\n"
    "v_mfma_f32_16x16x32_bf16 v[12:15], v[164:167], v[24:27], v[12:15]\n"
    "v_mfma_f32_16x16x32_bf16 v[12:15], v[168:171], v[24:27], v[12:15]\n"
    "v_mfma_f32_16x16x32_bf16 v[12:15], v[172:175], v[24:27], v[12:15]\n"
    "v_mfma_f32_16x16x32_bf16 v[12:15], v[176:179], v[24:27], v[12:15]\n"
    "v_mfma_f32_16x16x32_bf16 v[12:15], v[180:183], v[24:27], v[12:15]\n"
    "v_mfma_f32_16x16x32_bf16 v[12:15], v[184:187], v[24:27], v[12:15]\n"
    "v_mfma_f32_16x16x32_bf16 v[12:15], v[188:191], v[24:27], v[12:15]\n"
    "v_mfma_f32_16x16x32_bf16 v[16:19], v[192:195], v[24:27], 0\n"
    "v_mfma_f32_16x16x32_bf16 v[16:19], v[196:199], v[24:27], v[16:19]\n"
    "v_mfma_f32_16x16x32_bf16 v[16:19], v[200:203], v[24:27], v[16:19]\n"
    "v_mfma_f32_16x16x32_bf16 v[16:19], v[204:207], v[24:27], v[16:19]\n"
    "v_mfma_f32_16x16x32_bf16 v[16:19], v[208:211], v[24:27], v[16:19]\n"
    "v_mfma_f32_16x16x32_bf16 v[16:19], v[212:215], v[24:27], v[16:19]\n"
    "v_mfma_f32_16x16x32_bf16 v[16:19], v[216:219], v[24:27], v[16:19]\n"
    "v_mfma_f32_16x16x32_bf16 v[16:19], v[220:223], v[24:27], v[16:19]\n"
    "s_waitcnt vmcnt(11)\n"
    "v_mfma_f32_16x16x32_bf16 v[20:23], v[224:227], v[24:27], 0\n"
    "s_waitcnt vmcnt(10)\n"
    "v_mfma_f32_16x16x32_bf16 v[20:23], v[228:231], v[24:27], v[20:23]\n"
    "s_waitcnt vmcnt(9)\n"
    "v_mfma_f32_16x16x32_bf16 v[20:23], v[232:235], v[24:27], v[20:23]\n"
    "s_waitcnt vmcnt(8)\n"
    "v_mfma_f32_16x16x32_bf16 v[20:23], v[236:239], v[24:27], v[20:23]\n"
    "s_waitcnt vmcnt(7)\n"
    "v_mfma_f32_16x16x32_bf16 v[20:23], v[240:243], v[24:27], v[20:23]\n"
    "s_waitcnt vmcnt(6)\n"
    "v_mfma_f32_16x16x32_bf16 v[20:23], v[244:247], v[24:27], v[20:23]\n"
    "s_waitcnt vmcnt(5)\n"
    "v_mfma_f32_16x16x32_bf16 v[20:23], v[248:251], v[24:27], v[20:23]\n"
    "s_waitcnt vmcnt(4)\n"
    "v_mfma_f32_16x16x32_bf16 v[20:23], v[252:255], v[24:27], v[20:23]\n"
    "s_waitcnt vmcnt(3)\n"
    "v_max3_u32 v76, v92, v93, v94\n"
    "s_waitcnt vmcnt(2)\n"
    "v_max3_u32 v76, v76, v95, v96\n"
    "v_max3_u32 v76, v76, v97, v98\n"
    "s_waitcnt vmcnt(1)\n"
    "v_max3_u32 v76, v76, v99, v100\n"
    "v_max3_u32 v76, v76, v101, v102\n"
    "s_waitcnt vmcnt(0)\n"
    "v_max3_u32 v76, v76, v103, v104\n"
    "v_max3_u32 v76, v76, v105, v106\n"
    "v_max_u32 v76, v76, v107\n"
    "v_cmp_eq_u32 vcc, -1, v76\n"
    "s_nop 0\n"
    "s_cbranch_vccnz .Lsv_slow\n"
    ".Lsv_ok:\n"
    "s_waitcnt lgkmcnt(2)\n"
    "v_add_f32 v12, v12, v52\n"
    "v_add_f32 v13, v13, v53\n"
    "v_add_f32 v14, v14, v54\n"
    "v_add_f32 v15, v15, v55\n"
    "s_waitcnt lgkmcnt(1)\n"
    "v_add_f32 v16, v16, v56\n"
    "v_add_f32 v17, v17, v57\n"
    "v_add_f32 v18, v18, v58\n"
    "v_add_f32 v19, v19, v59\n"
    "s_waitcnt lgkmcnt(0)\n"
    "v_add_f32 v20, v20, v60\n"
    "v_add_f32 v21, v21, v61\n"
    "v_add_f32 v22, v22, v62\n"
    "v_add_f32 v23, v23, v63\n"
    "v_add_f32 v92, v92, v96\n"
    "v_add_f32 v93, v93, v97\n"
    "v_add_f32 v94, v94, v98\n"
    "v_add_f32 v95, v95, v99\n"
    "v_add_f32 v100, v100, v104\n"
    "v_add_f32 v101, v101, v105\n"
    "v_add_f32 v102, v102, v106\n"
    "v_add_f32 v103, v103, v107\n"
    "v_add_f32_dpp v92, v92, v92 row_ror:8 row_mask:0xf bank_mask:0xf\n"
    "v_add_f32_dpp v93, v93, v93 row_ror:8 row_mask:0xf bank_mask:0xf\n"
    "v_add_f32_dpp v94, v94, v94 row_ror:8 row_mask:0xf bank_mask:0xf\n"
    "v_add_f32_dpp v95, v95, v95 row_ror:8 row_mask:0xf bank_mask:0xf\n"
    "v_add_f32_dpp v100, v100, v100 row_ror:8 row_mask:0xf bank_mask:0xf\n"
    "v_add_f32_dpp v101, v101, v101 row_ror:8 row_mask:0xf bank_mask:0xf\n"
    "v_add_f32_dpp v102, v102, v102 row_ror:8 row_mask:0xf bank_mask:0xf\n"
    "v_add_f32_dpp v103, v103, v103 row_ror:8 row_mask:0xf bank_mask:0xf\n"
    "v_mov_b32 v96, v92\n"
    "v_mov_b32 v97, v93\n"
    "v_mov_b32 v98, v94\n"
    "v_mov_b32 v99, v95\n"
    "v_mov_b32 v104, v100\n"
    "v_mov_b32 v105, v101\n"
    "v_mov_b32 v106, v102\n"
    "v_mov_b32 v107, v103\n"
    "v_permlane16_swap_b32 v92, v96\n"
    "v_permlane16_swap_b32 v93, v97\n"
    "v_permlane16_swap_b32 v94, v98\n"
    "v_permlane16_swap_b32 v95, v99\n"
    "v_permlane16_swap_b32 v100, v104\n"
    "v_permlane16_swap_b32 v101, v105\n"
    "v_permlane16_swap_b32 v102, v106\n"
    "v_permlane16_swap_b32 v103, v107\n"
    "v_add_f32 v92, v92, v96\n"
    "v_add_f32 v93, v93, v97\n"
    "v_add_f32 v94, v94, v98\n"
    "v_add_f32 v95, v95, v99\n"
    "v_add_f32 v100, v100, v104\n"
    "v_add_f32 v101, v101, v105\n"
    "v_add_f32 v102, v102, v106\n"
    "v_add_f32 v103, v103, v107\n"
    "v_mov_b32 v96, v92\n"
    "v_mov_b32 v97, v93\n"
    "v_mov_b32 v98, v94\n"
    "v_mov_b32 v99, v95\n"
    "v_mov_b32 v104, v100\n"
    "v_mov_b32 v105, v101\n"
    "v_mov_b32 v106, v102\n"
    "v_mov_b32 v107, v103\n"
    "v_permlane32_swap_b32 v92, v96\n"
    "v_permlane32_swap_b32 v93, v97\n"
    "v_permlane32_swap_b32 v94, v98\n"
    "v_permlane32_swap_b32 v95, v99\n"
    "v_permlane32_swap_b32 v100, v104\n"
    "v_permlane32_swap_b32 v101, v105\n"
    "v_permlane32_swap_b32 v102, v106\n"
    "v_permlane32_swap_b32 v103, v107\n"
    "v_add_f32 v92, v92, v96\n"
    "v_add_f32 v93, v93, v97\n"
    "v_add_f32 v94, v94, v98\n"
    "v_add_f32 v95, v95, v99\n"
    "v_add_f32 v100, v100, v104\n"
    "v_add_f32 v101, v101, v105\n"
    "v_add_f32 v102, v102, v106\n"
    "v_add_f32 v103, v103, v107\n"
    "v_mul_f32 v108, 0x3b000000, v92\n"
    "v_mul_f32 v112, 0x3b000000, v94\n"
    "v_mul_f32 v116, 0x3b000000, v100\n"
    "v_mul_f32 v120, 0x3b000000, v102\n"
    "v_mul_f32 v93, 0x3b000000, v93\n"
    "v_mul_f32 v95, 0x3b000000, v95\n"
    "v_mul_f32 v101, 0x3b000000, v101\n"
    "v_mul_f32 v103, 0x3b000000, v103\n"
    "v_fma_f32 v93, -v108, v108, v93\n"
    "v_fma_f32 v95, -v112, v112, v95\n"
    "v_fma_f32 v101, -v116, v116, v101\n"
    "v_fma_f32 v103, -v120, v120, v103\n"
    "v_max_f32 v93, 0, v93\n"
    "v_max_f32 v95, 0, v95\n"
    "v_max_f32 v101, 0, v101\n"
    "v_max_f32 v103, 0, v103\n"
    "v_add_f32 v111, 0x3727c5ac, v93\n"
    "v_add_f32 v115, 0x3727c5ac, v95\n"
    "v_add_f32 v119, 0x3727c5ac, v101\n"
    "v_add_f32 v123, 0x3727c5ac, v103\n"
    "v_rsq_f32 v92, v111\n"
    "v_rsq_f32 v94, v115\n"
    "v_rsq_f32 v100, v119\n"
    "v_rsq_f32 v102, v123\n"
    "v_mul_f32 v109, 0xc0135761, v92\n"
    "v_mul_f32 v113, 0xc0135761, v94\n"
    "v_mul_f32 v117, 0xc0135761, v100\n"
    "v_mul_f32 v121, 0xc0135761, v102\n"
    "v_mul_f32 v93, v92, v92\n"
    "v_mul_f32 v95, v94, v94\n"
    "v_mul_f32 v101, v100, v100\n"
    "v_mul_f32 v103, v102, v102\n"
    "v_mul_f32 v93, 0x3d372713, v93\n"
    "v_mul_f32 v95, 0x3d372713, v95\n"
    "v_mul_f32 v101, 0x3d372713, v101\n"
    "v_mul_f32 v103, 0x3d372713, v103\n"
    "v_mul_f32 v110, v109, v93\n"
    "v_mul_f32 v114, v113, v95\n"
    "v_mul_f32 v118, v117, v101\n"
    "v_mul_f32 v122, v121, v103\n"
    "s_lshl_b32 s60, s53, 9\n"
    "s_add_u32 s60, s60, 0x6000\n"
    "v_lshlrev_b32 v77, 5, v0\n"
    "v_add_u32 v77, s60, v77\n"
    "s_mov_b64 exec, 0xff\n"
    "ds_write_b128 v77, v[108:111]\n"
    "ds_write_b128 v77, v[112:115] offset:16\n"
    "ds_write_b128 v77, v[116:119] offset:256\n"
    "ds_write_b128 v77, v[120:123] offset:272\n"
    "s_mov_b64 exec, -1\n"
    "s_waitcnt lgkmcnt(0)\n"
    "s_barrier\n"
    "ds_read_b128 v[52:55], v8\n"
    "ds_read_b128 v[56:59], v8 offset:1024\n"
    "ds_read_b128 v[60:63], v8 offset:2048\n"
    "ds_read_b128 v[28:31], v9\n"
    "ds_read_b128 v[32:35], v9 offset:1024\n"
    "ds_read_b128 v[36:39], v9 offset:2048\n"
    "ds_read_b128 v[40:43], v9 offset:3072\n"
    "s_waitcnt lgkmcnt(3)\n"
    "v_mfma_f32_16x16x32_bf16 v[52:55], v[160:163], v[28:31], v[52:55]\n"
    "v_mfma_f32_16x16x32_bf16 v[56:59], v[192:195], v[28:31], v[56:59]\n"
    "v_mfma_f32_16x16x32_bf16 v[60:63], v[224:227], v[28:31], v[60:63]\n"
    "ds_read_b128 v[44:47], v9 offset:4096\n"
    "s_waitcnt lgkmcnt(3)\n"
    "v_mfma_f32_16x16x32_bf16 v[52:55], v[164:167], v[32:35], v[52:55]\n"
    "v_mfma_f32_16x16x32_bf16 v[56:59], v[196:199], v[32:35], v[56:59]\n"
    "v_mfma_f32_16x16x32_bf16 v[60:63], v[228:231], v[32:35], v[60:63]\n"
    "ds_read_b128 v[48:51], v9 offset:5120\n"
    "s_waitcnt lgkmcnt(3)\n"
    "v_mfma_f32_16x16x32_bf16 v[52:55], v[168:171], v[36:39], v[52:55]\n"
    "v_mfma_f32_16x16x32_bf16 v[56:59], v[200:203], v[36:39], v[56:59]\n"
    "v_mfma_f32_16x16x32_bf16 v[60:63], v[232:235], v[36:39], v[60:63]\n"
    "ds_read_b128 v[28:31], v9 offset:6144\n"
    "s_waitcnt lgkmcnt(3)\n"
    "v_mfma_f32_16x16x32_bf16 v[52:55], v[172:175], v[40:43], v[52:55]\n"
    "v_mfma_f32_16x16x32_bf16 v[56:59], v[204:207], v[40:43], v[56:59]\n"
    "v_mfma_f32_16x16x32_bf16 v[60:63], v[236:239], v[40:43], v[60:63]\n"
    "ds_read_b128 v[32:35], v9 offset:7168\n"
    "s_waitcnt lgkmcnt(3)\n"
    "v_mfma_f32_16x16x32_bf16 v[52:55], v[176:179], v[44:47], v[52:55]\n"
    "v_mfma_f32_16x16x32_bf16 v[56:59], v[208:211], v[44:47], v[56:59]\n"
    "v_mfma_f32_16x16x32_bf16 v[60:63], v[240:243], v[44:47], v[60:63]\n"
    "s_waitcnt lgkmcnt(2)\n"
    "v_mfma_f32_16x16x32_bf16 v[52:55], v[180:183], v[48:51], v[52:55]\n"
    "v_mfma_f32_16x16x32_bf16 v[56:59], v[212:215], v[48:51], v[56:59]\n"
    "v_mfma_f32_16x16x32_bf16 v[60:63], v[244:247], v[48:51], v[60:63]\n"
    "s_waitcnt lgkmcnt(1)\n"
    "v_mfma_f32_16x16x32_bf16 v[52:55], v[184:187], v[28:31], v[52:55]\n"
    "v_mfma_f32_16x16x32_bf16 v[56:59], v[216:219], v[28:31], v[56:59]\n"
    "v_mfma_f32_16x16x32_bf16 v[60:63], v[248:251], v[28:31], v[60:63]\n"
    "s_waitcnt lgkmcnt(0)\n"
    "v_mfma_f32_16x16x32_bf16 v[52:55], v[188:191], v[32:35], v[52:55]\n"
    "v_mfma_f32_16x16x32_bf16 v[56:59], v[220:223], v[32:35], v[56:59]\n"
    "v_mfma_f32_16x16x32_bf16 v[60:63], v[252:255], v[32:35], v[60:63]\n"
    "s_barrier\n"
    "ds_read_b128 v[64:67], v8 offset:12288\n"
    "ds_read_b128 v[68:71], v8 offset:13312\n"
    "ds_read_b128 v[72:75], v8 offset:14336\n"
    "ds_read_b128 v[36:39], v9 offset:8192\n"
    "ds_read_b128 v[40:43], v9 offset:9216\n"
    "ds_read_b128 v[44:47], v9 offset:10240\n"
    "ds_read_b128 v[48:51], v9 offset:11264\n"
    "ds_read_b128 v[84:87], v11\n"
    "s_waitcnt lgkmcnt(0)\n"
    "v_fma_f32 v76, -v84, v12, v52\n"
    "v_fma_f32 v79, -v84, v13, v53\n"
    "v_mul_f32 v78, v76, v76\n"
    "v_mul_f32 v81, v79, v79\n"
    "v_fma_f32 v78, v78, v86, v85\n"
    "v_fma_f32 v81, v81, v86, v85\n"
    "v_mul_f32 v78, v76, v78\n"
    "v_mul_f32 v81, v79, v81\n"
    "v_exp_f32 v78, v78\n"
    "v_exp_f32 v81, v81\n"
    "v_fma_f32 v77, -v84, v14, v54\n"
    "v_fma_f32 v80, -v84, v15, v55\n"
    "v_mul_f32 v76, v76, v77\n"
    "v_mul_f32 v79, v79, v80\n"
    "v_fma_f32 v78, v78, v87, v87\n"
    "v_fma_f32 v81, v81, v87, v87\n"
    "v_rcp_f32 v78, v78\n"
    "v_rcp_f32 v81, v81\n"
    "v_mul_f32 v76, v76, v78\n"
    "v_mul_f32 v79, v79, v81\n"
    "v_cvt_pk_bf16_f32 v88, v76, v79\n"
    "v_fma_f32 v76, -v84, v16, v56\n"
    "v_fma_f32 v79, -v84, v17, v57\n"
    "v_mul_f32 v78, v76, v76\n"
    "v_mul_f32 v81, v79, v79\n"
    "v_fma_f32 v78, v78, v86, v85\n"
    "v_fma_f32 v81, v81, v86, v85\n"
    "v_mul_f32 v78, v76, v78\n"
    "v_mul_f32 v81, v79, v81\n"
    "v_exp_f32 v78, v78\n"
    "v_exp_f32 v81, v81\n"
    "v_fma_f32 v77, -v84, v18, v58\n"
    "v_fma_f32 v80, -v84, v19, v59\n"
    "v_mul_f32 v76, v76, v77\n"
    "v_mul_f32 v79, v79, v80\n"
    "v_fma_f32 v78, v78, v87, v87\n"
    "v_fma_f32 v81, v81, v87, v87\n"
    "v_rcp_f32 v78, v78\n"
    "v_rcp_f32 v81, v81\n"
    "v_mul_f32 v76, v76, v78\n"
    "v_mul_f32 v79, v79, v81\n"
    "v_cvt_pk_bf16_f32 v89, v76, v79\n"
    "v_fma_f32 v76, -v84, v20, v60\n"
    "v_mfma_f32_16x16x32_bf16 v[64:67], v[160:163], v[36:39], v[64:67]\n"
    "v_fma_f32 v79, -v84, v21, v61\n"
    "v_mfma_f32_16x16x32_bf16 v[68:71], v[192:195], v[36:39], v[68:71]\n"
    "v_mul_f32 v78, v76, v76\n"
    "v_mfma_f32_16x16x32_bf16 v[72:75], v[224:227], v[36:39], v[72:75]\n"
    "v_mul_f32 v81, v79, v79\n"
    "ds_read_b128 v[28:31], v9 offset:12288\n"
    "v_mfma_f32_16x16x32_bf16 v[64:67], v[164:167], v[40:43], v[64:67]\n"
    "v_fma_f32 v78, v78, v86, v85\n"
    "v_mfma_f32_16x16x32_bf16 v[68:71], v[196:199], v[40:43], v[68:71]\n"
    "v_fma_f32 v81, v81, v86, v85\n"
    "v_mfma_f32_16x16x32_bf16 v[72:75], v[228:231], v[40:43], v[72:75]\n"
    "v_mul_f32 v78, v76, v78\n"
    "ds_read_b128 v[32:35], v9 offset:13312\n"
    "v_mfma_f32_16x16x32_bf16 v[64:67], v[168:171], v[44:47], v[64:67]\n"
    "v_mul_f32 v81, v79, v81\n"
    "v_mfma_f32_16x16x32_bf16 v[68:71], v[200:203], v[44:47], v[68:71]\n"
    "v_exp_f32 v78, v78\n"
    "v_mfma_f32_16x16x32_bf16 v[72:75], v[232:235], v[44:47], v[72:75]\n"
    "v_exp_f32 v81, v81\n"
    "ds_read_b128 v[36:39], v9 offset:14336\n"
    "v_mfma_f32_16x16x32_bf16 v[64:67], v[172:175], v[48:51], v[64:67]\n"
    "v_fma_f32 v77, -v84, v22, v62\n"
    "v_mfma_f32_16x16x32_bf16 v[68:71], v[204:207], v[48:51], v[68:71]\n"
    "v_fma_f32 v80, -v84, v23, v63\n"
    "v_mfma_f32_16x16x32_bf16 v[72:75], v[236:239], v[48:51], v[72:75]\n"
    "v_mul_f32 v76, v76, v77\n"
    "ds_read_b128 v[40:43], v9 offset:15360\n"
    "s_waitcnt lgkmcnt(3)\n"
    "v_mfma_f32_16x16x32_bf16 v[64:67], v[176:179], v[28:31], v[64:67]\n"
    "v_mul_f32 v79, v79, v80\n"
    "v_mfma_f32_16x16x32_bf16 v[68:71], v[208:211], v[28:31], v[68:71]\n"
    "v_fma_f32 v78, v78, v87, v87\n"
    "v_mfma_f32_16x16x32_bf16 v[72:75], v[240:243], v[28:31], v[72:75]\n"
    "v_fma_f32 v81, v81, v87, v87\n"
    "ds_read_b128 v[44:47], v9 offset:16384\n"
    "s_waitcnt lgkmcnt(3)\n"
    "v_mfma_f32_16x16x32_bf16 v[64:67], v[180:183], v[32:35], v[64:67]\n"
    "v_rcp_f32 v78, v78\n"
    "v_mfma_f32_16x16x32_bf16 v[68:71], v[212:215], v[32:35], v[68:71]\n"
    "v_rcp_f32 v81, v81\n"
    "v_mfma_f32_16x16x32_bf16 v[72:75], v[244:247], v[32:35], v[72:75]\n"
    "v_mul_f32 v76, v76, v78\n"
    "ds_read_b128 v[48:51], v9 offset:17408\n"
    "s_waitcnt lgkmcnt(3)\n"
    "v_mfma_f32_16x16x32_bf16 v[64:67], v[184:187], v[36:39], v[64:67]\n"
    "v_mul_f32 v79, v79, v81\n"
    "v_mfma_f32_16x16x32_bf16 v[68:71], v[216:219], v[36:39], v[68:71]\n"
    "v_cvt_pk_bf16_f32 v90, v76, v79\n"
    "v_mfma_f32_16x16x32_bf16 v[72:75], v[248:251], v[36:39], v[72:75]\n"
    "v_permlane16_swap_b32 v88, v89\n"
    "ds_read_b128 v[28:31], v9 offset:18432\n"
    "s_waitcnt lgkmcnt(3)\n"
    "v_mfma_f32_16x16x32_bf16 v[64:67], v[188:191], v[40:43], v[64:67]\n"
    "v_permlane16_swap_b32 v90, v91\n"
    "v_mfma_f32_16x16x32_bf16 v[68:71], v[220:223], v[40:43], v[68:71]\n"
    "s_nop 0\n"
    "v_permlane32_swap_b32 v88, v90\n"
    "v_mfma_f32_16x16x32_bf16 v[72:75], v[252:255], v[40:43], v[72:75]\n"
    "v_permlane32_swap_b32 v89, v91\n"
    "ds_read_b128 v[32:35], v9 offset:19456\n"
    "buffer_store_dwordx4 v[88:91], v10, s[36:39], s69 offen sc1\n"
    "s_add_u32 s69, s69, 0xac00\n"
    "s_barrier\n"
    "ds_read_b128 v[52:55], v8\n"
    "ds_read_b128 v[56:59], v8 offset:1024\n"
    "ds_read_b128 v[60:63], v8 offset:2048\n"
    "ds_read_b128 v[124:127], v11 offset:256\n"
    "s_waitcnt lgkmcnt(0)\n"
    "v_fma_f32 v76, -v124, v12, v64\n"
    "v_fma_f32 v79, -v124, v13, v65\n"
    "v_mul_f32 v78, v76, v76\n"
    "v_mul_f32 v81, v79, v79\n"
    "v_fma_f32 v78, v78, v126, v125\n"
    "v_fma_f32 v81, v81, v126, v125\n"
    "v_mul_f32 v78, v76, v78\n"
    "v_mul_f32 v81, v79, v81\n"
    "v_exp_f32 v78, v78\n"
    "v_exp_f32 v81, v81\n"
    "v_fma_f32 v77, -v124, v14, v66\n"
    "v_fma_f32 v80, -v124, v15, v67\n"
    "v_mul_f32 v76, v76, v77\n"
    "v_mul_f32 v79, v79, v80\n"
    "v_fma_f32 v78, v78, v127, v127\n"
    "v_fma_f32 v81, v81, v127, v127\n"
    "v_rcp_f32 v78, v78\n"
    "v_rcp_f32 v81, v81\n"
    "v_mul_f32 v76, v76, v78\n"
    "v_mul_f32 v79, v79, v81\n"
    "v_cvt_pk_bf16_f32 v88, v76, v79\n"
    "v_fma_f32 v76, -v124, v16, v68\n"
    "v_fma_f32 v79, -v124, v17, v69\n"
    "v_mul_f32 v78, v76, v76\n"
    "v_mul_f32 v81, v79, v79\n"
    "v_fma_f32 v78, v78, v126, v125\n"
    "v_fma_f32 v81, v81, v126, v125\n"
    "v_mul_f32 v78, v76, v78\n"
    "v_mul_f32 v81, v79, v81\n"
    "v_exp_f32 v78, v78\n"
    "v_exp_f32 v81, v81\n"
    "v_fma_f32 v77, -v124, v18, v70\n"
    "v_fma_f32 v80, -v124, v19, v71\n"
    "v_mul_f32 v76, v76, v77\n"
    "v_mul_f32 v79, v79, v80\n"
    "v_fma_f32 v78, v78, v127, v127\n"
    "v_fma_f32 v81, v81, v127, v127\n"
    "v_rcp_f32 v78, v78\n"
    "v_rcp_f32 v81, v81\n"
    "v_mul_f32 v76, v76, v78\n"
    "v_mul_f32 v79, v79, v81\n"
    "v_cvt_pk_bf16_f32 v89, v76, v79\n"
    "v_fma_f32 v76, -v124, v20, v72\n"
    "v_mfma_f32_16x16x32_bf16 v[52:55], v[160:163], v[44:47], v[52:55]\n"
    "v_fma_f32 v79, -v124, v21, v73\n"
    "v_mfma_f32_16x16x32_bf16 v[56:59], v[192:195], v[44:47], v[56:59]\n"
    "v_mul_f32 v78, v76, v76\n"
    "v_mfma_f32_16x16x32_bf16 v[60:63], v[224:227], v[44:47], v[60:63]\n"
    "v_mul_f32 v81, v79, v79\n"
    "ds_read_b128 v[36:39], v9 offset:20480\n"
    "v_mfma_f32_16x16x32_bf16 v[52:55], v[164:167], v[48:51], v[52:55]\n"
    "v_fma_f32 v78, v78, v126, v125\n"
    "v_mfma_f32_16x16x32_bf16 v[56:59], v[196:199], v[48:51], v[56:59]\n"
    "v_fma_f32 v81, v81, v126, v125\n"
    "v_mfma_f32_16x16x32_bf16 v[60:63], v[228:231], v[48:51], v[60:63]\n"
    "v_mul_f32 v78, v76, v78\n"
    "ds_read_b128 v[40:43], v9 offset:21504\n"
    "v_mfma_f32_16x16x32_bf16 v[52:55], v[168:171], v[28:31], v[52:55]\n"
    "v_mul_f32 v81, v79, v81\n"
    "v_mfma_f32_16x16x32_bf16 v[56:59], v[200:203], v[28:31], v[56:59]\n"
    "v_exp_f32 v78, v78\n"
    "v_mfma_f32_16x16x32_bf16 v[60:63], v[232:235], v[28:31], v[60:63]\n"
    "v_exp_f32 v81, v81\n"
    "ds_read_b128 v[44:47], v9 offset:22528\n"
    "v_mfma_f32_16x16x32_bf16 v[52:55], v[172:175], v[32:35], v[52:55]\n"
    "v_fma_f32 v77, -v124, v22, v74\n"
    "v_mfma_f32_16x16x32_bf16 v[56:59], v[204:207], v[32:35], v[56:59]\n"
    "v_fma_f32 v80, -v124, v23, v75\n"
    "v_mfma_f32_16x16x32_bf16 v[60:63], v[236:239], v[32:35], v[60:63]\n"
    "v_mul_f32 v76, v76, v77\n"
    "ds_read_b128 v[48:51], v9 offset:23552\n"
    "s_waitcnt lgkmcnt(3)\n"
    "v_mfma_f32_16x16x32_bf16 v[52:55], v[176:179], v[36:39], v[52:55]\n"
    "v_mul_f32 v79, v79, v80\n"
    "v_mfma_f32_16x16x32_bf16 v[56:59], v[208:211], v[36:39], v[56:59]\n"
    "v_fma_f32 v78, v78, v127, v127\n"
    "v_mfma_f32_16x16x32_bf16 v[60:63], v[240:243], v[36:39], v[60:63]\n"
    "v_fma_f32 v81, v81, v127, v127\n"
    "ds_read_b128 v[28:31], v9 offset:24576\n"
    "s_waitcnt lgkmcnt(3)\n"
    "v_mfma_f32_16x16x32_bf16 v[52:55], v[180:183], v[40:43], v[52:55]\n"
    "v_rcp_f32 v78, v78\n"
    "v_mfma_f32_16x16x32_bf16 v[56:59], v[212:215], v[40:43], v[56:59]\n"
    "v_rcp_f32 v81, v81\n"
    "v_mfma_f32_16x16x32_bf16 v[60:63], v[244:247], v[40:43], v[60:63]\n"
    "v_mul_f32 v76, v76, v78\n"
    "ds_read_b128 v[32:35], v9 offset:25600\n"
    "s_waitcnt lgkmcnt(3)\n"
    "v_mfma_f32_16x16x32_bf16 v[52:55], v[184:187], v[44:47], v[52:55]\n"
    "v_mul_f32 v79, v79, v81\n"
    "v_mfma_f32_16x16x32_bf16 v[56:59], v[216:219], v[44:47], v[56:59]\n"
    "v_cvt_pk_bf16_f32 v90, v76, v79\n"
    "v_mfma_f32_16x16x32_bf16 v[60:63], v[248:251], v[44:47], v[60:63]\n"
    "v_permlane16_swap_b32 v88, v89\n"
    "ds_read_b128 v[36:39], v9 offset:26624\n"
    "s_waitcnt lgkmcnt(3)\n"
    "v_mfma_f32_16x16x32_bf16 v[52:55], v[188:191], v[48:51], v[52:55]\n"
    "v_permlane16_swap_b32 v90, v91\n"
    "v_mfma_f32_16x16x32_bf16 v[56:59], v[220:223], v[48:51], v[56:59]\n"
    "s_nop 0\n"
    "v_permlane32_swap_b32 v88, v90\n"
    "v_mfma_f32_16x16x32_bf16 v[60:63], v[252:255], v[48:51], v[60:63]\n"
    "v_permlane32_swap_b32 v89, v91\n"
    "ds_read_b128 v[40:43], v9 offset:27648\n"
    "buffer_store_dwordx4 v[88:91], v10, s[36:39], s69 offen sc1\n"
    "s_add_u32 s69, s69, 0xac00\n"
    "s_barrier\n"
    "ds_read_b128 v[64:67], v8 offset:12288\n"
    "ds_read_b128 v[68:71], v8 offset:13312\n"
    "ds_read_b128 v[72:75], v8 offset:14336\n"
    "ds_read_b128 v[84:87], v11 offset:512\n"
    "s_waitcnt lgkmcnt(0)\n"
    "v_fma_f32 v76, -v84, v12, v52\n"
    "v_fma_f32 v79, -v84, v13, v53\n"
    "v_mul_f32 v78, v76, v76\n"
    "v_mul_f32 v81, v79, v79\n"
    "v_fma_f32 v78, v78, v86, v85\n"
    "v_fma_f32 v81, v81, v86, v85\n"
    "v_mul_f32 v78, v76, v78\n"
    "v_mul_f32 v81, v79, v81\n"
    "v_exp_f32 v78, v78\n"
    "v_exp_f32 v81, v81\n"
    "v_fma_f32 v77, -v84, v14, v54\n"
    "v_fma_f32 v80, -v84, v15, v55\n"
    "v_mul_f32 v76, v76, v77\n"
    "v_mul_f32 v79, v79, v80\n"
    "v_fma_f32 v78, v78, v87, v87\n"
    "v_fma_f32 v81, v81, v87, v87\n"
    "v_rcp_f32 v78, v78\n"
    "v_rcp_f32 v81, v81\n"
    "v_mul_f32 v76, v76, v78\n"
    "v_mul_f32 v79, v79, v81\n"
    "v_cvt_pk_bf16_f32 v88, v76, v79\n"
    "v_fma_f32 v76, -v84, v16, v56\n"
    "v_fma_f32 v79, -v84, v17, v57\n"
    "v_mul_f32 v78, v76, v76\n"
    "v_mul_f32 v81, v79, v79\n"
    "v_fma_f32 v78, v78, v86, v85\n"
    "v_fma_f32 v81, v81, v86, v85\n"
    "v_mul_f32 v78, v76, v78\n"
    "v_mul_f32 v81, v79, v81\n"
    "v_exp_f32 v78, v78\n"
    "v_exp_f32 v81, v81\n"
    "v_fma_f32 v77, -v84, v18, v58\n"
    "v_fma_f32 v80, -v84, v19, v59\n"
    "v_mul_f32 v76, v76, v77\n"
    "v_mul_f32 v79, v79, v80\n"
    "v_fma_f32 v78, v78, v87, v87\n"
    "v_fma_f32 v81, v81, v87, v87\n"
    "v_rcp_f32 v78, v78\n"
    "v_rcp_f32 v81, v81\n"
    "v_mul_f32 v76, v76, v78\n"
    "v_mul_f32 v79, v79, v81\n"
    "v_cvt_pk_bf16_f32 v89, v76, v79\n"
    "v_fma_f32 v76, -v84, v20, v60\n"
    "v_mfma_f32_16x16x32_bf16 v[64:67], v[160:163], v[28:31], v[64:67]\n"
    "v_fma_f32 v79, -v84, v21, v61\n"
    "v_mfma_f32_16x16x32_bf16 v[68:71], v[192:195], v[28:31], v[68:71]\n"
    "v_mul_f32 v78, v76, v76\n"
    "v_mfma_f32_16x16x32_bf16 v[72:75], v[224:227], v[28:31], v[72:75]\n"
    "v_mul_f32 v81, v79, v79\n"
    "ds_read_b128 v[44:47], v9 offset:28672\n"
    "v_mfma_f32_16x16x32_bf16 v[64:67], v[164:167], v[32:35], v[64:67]\n"
    "v_fma_f32 v78, v78, v86, v85\n"
    "v_mfma_f32_16x16x32_bf16 v[68:71], v[196:199], v[32:35], v[68:71]\n"
    "v_fma_f32 v81, v81, v86, v85\n"
    "v_mfma_f32_16x16x32_bf16 v[72:75], v[228:231], v[32:35], v[72:75]\n"
    "v_mul_f32 v78, v76, v78\n"
    "ds_read_b128 v[48:51], v9 offset:29696\n"
    "v_mfma_f32_16x16x32_bf16 v[64:67], v[168:171], v[36:39], v[64:67]\n"
    "v_mul_f32 v81, v79, v81\n"
    "v_mfma_f32_16x16x32_bf16 v[68:71], v[200:203], v[36:39], v[68:71]\n"
    "v_exp_f32 v78, v78\n"
    "v_mfma_f32_16x16x32_bf16 v[72:75], v[232:235], v[36:39], v[72:75]\n"
    "v_exp_f32 v81, v81\n"
    "ds_read_b128 v[28:31], v9 offset:30720\n"
    "v_mfma_f32_16x16x32_bf16 v[64:67], v[172:175], v[40:43], v[64:67]\n"
    "v_fma_f32 v77, -v84, v22, v62\n"
    "v_mfma_f32_16x16x32_bf16 v[68:71], v[204:207], v[40:43], v[68:71]\n"
    "v_fma_f32 v80, -v84, v23, v63\n"
    "v_mfma_f32_16x16x32_bf16 v[72:75], v[236:239], v[40:43], v[72:75]\n"
    "v_mul_f32 v76, v76, v77\n"
    "ds_read_b128 v[32:35], v9 offset:31744\n"
    "s_waitcnt lgkmcnt(3)\n"
    "v_mfma_f32_16x16x32_bf16 v[64:67], v[176:179], v[44:47], v[64:67]\n"
    "v_mul_f32 v79, v79, v80\n"
    "v_mfma_f32_16x16x32_bf16 v[68:71], v[208:211], v[44:47], v[68:71]\n"
    "v_fma_f32 v78, v78, v87, v87\n"
    "v_mfma_f32_16x16x32_bf16 v[72:75], v[240:243], v[44:47], v[72:75]\n"
    "v_fma_f32 v81, v81, v87, v87\n"
    "ds_read_b128 v[36:39], v9\n"
    "s_waitcnt lgkmcnt(3)\n"
    "v_mfma_f32_16x16x32_bf16 v[64:67], v[180:183], v[48:51], v[64:67]\n"
    "v_rcp_f32 v78, v78\n"
    "v_mfma_f32_16x16x32_bf16 v[68:71], v[212:215], v[48:51], v[68:71]\n"
    "v_rcp_f32 v81, v81\n"
    "v_mfma_f32_16x16x32_bf16 v[72:75], v[244:247], v[48:51], v[72:75]\n"
    "v_mul_f32 v76, v76, v78\n"
    "ds_read_b128 v[40:43], v9 offset:1024\n"
    "s_waitcnt lgkmcnt(3)\n"
    "v_mfma_f32_16x16x32_bf16 v[64:67], v[184:187], v[28:31], v[64:67]\n"
    "v_mul_f32 v79, v79, v81\n"
    "v_mfma_f32_16x16x32_bf16 v[68:71], v[216:219], v[28:31], v[68:71]\n"
    "v_cvt_pk_bf16_f32 v90, v76, v79\n"
    "v_mfma_f32_16x16x32_bf16 v[72:75], v[248:251], v[28:31], v[72:75]\n"
    "v_permlane16_swap_b32 v88, v89\n"
    "ds_read_b128 v[44:47], v9 offset:2048\n"
    "s_waitcnt lgkmcnt(3)\n"
    "v_mfma_f32_16x16x32_bf16 v[64:67], v[188:191], v[32:35], v[64:67]\n"
    "v_permlane16_swap_b32 v90, v91\n"
    "v_mfma_f32_16x16x32_bf16 v[68:71], v[220:223], v[32:35], v[68:71]\n"
    "s_nop 0\n"
    "v_permlane32_swap_b32 v88, v90\n"
    "v_mfma_f32_16x16x32_bf16 v[72:75], v[252:255], v[32:35], v[72:75]\n"
    "v_permlane32_swap_b32 v89, v91\n"
    "ds_read_b128 v[48:51], v9 offset:3072\n"
    "buffer_store_dwordx4 v[88:91], v10, s[36:39], s69 offen sc1\n"
    "s_add_u32 s69, s69, 0xac00\n"
    "s_barrier\n"
    "ds_read_b128 v[52:55], v8\n"
    "ds_read_b128 v[56:59], v8 offset:1024\n"
    "ds_read_b128 v[60:63], v8 offset:2048\n"
    "ds_read_b128 v[124:127], v11 offset:768\n"
    "s_waitcnt lgkmcnt(0)\n"
    "v_fma_f32 v76, -v124, v12, v64\n"
    "v_fma_f32 v79, -v124, v13, v65\n"
    "v_mul_f32 v78, v76, v76\n"
    "v_mul_f32 v81, v79, v79\n"
    "v_fma_f32 v78, v78, v126, v125\n"
    "v_fma_f32 v81, v81, v126, v125\n"
    "v_mul_f32 v78, v76, v78\n"
    "v_mul_f32 v81, v79, v81\n"
    "v_exp_f32 v78, v78\n"
    "v_exp_f32 v81, v81\n"
    "v_fma_f32 v77, -v124, v14, v66\n"
    "v_fma_f32 v80, -v124, v15, v67\n"
    "v_mul_f32 v76, v76, v77\n"
    "v_mul_f32 v79, v79, v80\n"
    "v_fma_f32 v78, v78, v127, v127\n"
    "v_fma_f32 v81, v81, v127, v127\n"
    "v_rcp_f32 v78, v78\n"
    "v_rcp_f32 v81, v81\n"
    "v_mul_f32 v76, v76, v78\n"
    "v_mul_f32 v79, v79, v81\n"
    "v_cvt_pk_bf16_f32 v88, v76, v79\n"
    "v_fma_f32 v76, -v124, v16, v68\n"
    "v_fma_f32 v79, -v124, v17, v69\n"
    "v_mul_f32 v78, v76, v76\n"
    "v_mul_f32 v81, v79, v79\n"
    "v_fma_f32 v78, v78, v126, v125\n"
    "v_fma_f32 v81, v81, v126, v125\n"
    "v_mul_f32 v78, v76, v78\n"
    "v_mul_f32 v81, v79, v81\n"
    "v_exp_f32 v78, v78\n"
    "v_exp_f32 v81, v81\n"
    "v_fma_f32 v77, -v124, v18, v70\n"
    "v_fma_f32 v80, -v124, v19, v71\n"
    "v_mul_f32 v76, v76, v77\n"
    "v_mul_f32 v79, v79, v80\n"
    "v_fma_f32 v78, v78, v127, v127\n"
    "v_fma_f32 v81, v81, v127, v127\n"
    "v_rcp_f32 v78, v78\n"
    "v_rcp_f32 v81, v81\n"
    "v_mul_f32 v76, v76, v78\n"
    "v_mul_f32 v79, v79, v81\n"
    "v_cvt_pk_bf16_f32 v89, v76, v79\n"
    "v_fma_f32 v76, -v124, v20, v72\n"
    "v_mfma_f32_16x16x32_bf16 v[52:55], v[160:163], v[36:39], v[52:55]\n"
    "v_fma_f32 v79, -v124, v21, v73\n"
    "v_mfma_f32_16x16x32_bf16 v[56:59], v[192:195], v[36:39], v[56:59]\n"
    "v_mul_f32 v78, v76, v76\n"
    "v_mfma_f32_16x16x32_bf16 v[60:63], v[224:227], v[36:39], v[60:63]\n"
    "v_mul_f32 v81, v79, v79\n"
    "ds_read_b128 v[28:31], v9 offset:4096\n"
    "v_mfma_f32_16x16x32_bf16 v[52:55], v[164:167], v[40:43], v[52:55]\n"
    "v_fma_f32 v78, v78, v126, v125\n"
    "v_mfma_f32_16x16x32_bf16 v[56:59], v[196:199], v[40:43], v[56:59]\n"
    "v_fma_f32 v81, v81, v126, v125\n"
    "v_mfma_f32_16x16x32_bf16 v[60:63], v[228:231], v[40:43], v[60:63]\n"
    "v_mul_f32 v78, v76, v78\n"
    "ds_read_b128 v[32:35], v9 offset:5120\n"
    "v_mfma_f32_16x16x32_bf16 v[52:55], v[168:171], v[44:47], v[52:55]\n"
    "v_mul_f32 v81, v79, v81\n"
    "v_mfma_f32_16x16x32_bf16 v[56:59], v[200:203], v[44:47], v[56:59]\n"
    "v_exp_f32 v78, v78\n"
    "v_mfma_f32_16x16x32_bf16 v[60:63], v[232:235], v[44:47], v[60:63]\n"
    "v_exp_f32 v81, v81\n"
    "ds_read_b128 v[36:39], v9 offset:6144\n"
    "v_mfma_f32_16x16x32_bf16 v[52:55], v[172:175], v[48:51], v[52:55]\n"
    "v_fma_f32 v77, -v124, v22, v74\n"
    "v_mfma_f32_16x16x32_bf16 v[56:59], v[204:207], v[48:51], v[56:59]\n"
    "v_fma_f32 v80, -v124, v23, v75\n"
    "v_mfma_f32_16x16x32_bf16 v[60:63], v[236:239], v[48:51], v[60:63]\n"
    "v_mul_f32 v76, v76, v77\n"
    "ds_read_b128 v[40:43], v9 offset:7168\n"
    "s_waitcnt lgkmcnt(3)\n"
    "v_mfma_f32_16x16x32_bf16 v[52:55], v[176:179], v[28:31], v[52:55]\n"
    "v_mul_f32 v79, v79, v80\n"
    "v_mfma_f32_16x16x32_bf16 v[56:59], v[208:211], v[28:31], v[56:59]\n"
    "v_fma_f32 v78, v78, v127, v127\n"
    "v_mfma_f32_16x16x32_bf16 v[60:63], v[240:243], v[28:31], v[60:63]\n"
    "v_fma_f32 v81, v81, v127, v127\n"
    "ds_read_b128 v[44:47], v9 offset:8192\n"
    "s_waitcnt lgkmcnt(3)\n"
    "v_mfma_f32_16x16x32_bf16 v[52:55], v[180:183], v[32:35], v[52:55]\n"
    "v_rcp_f32 v78, v78\n"
    "v_mfma_f32_16x16x32_bf16 v[56:59], v[212:215], v[32:35], v[56:59]\n"
    "v_rcp_f32 v81, v81\n"
    "v_mfma_f32_16x16x32_bf16 v[60:63], v[244:247], v[32:35], v[60:63]\n"
    "v_mul_f32 v76, v76, v78\n"
    "ds_read_b128 v[48:51], v9 offset:9216\n"
    "s_waitcnt lgkmcnt(3)\n"
    "v_mfma_f32_16x16x32_bf16 v[52:55], v[184:187], v[36:39], v[52:55]\n"
    "v_mul_f32 v79, v79, v81\n"
    "v_mfma_f32_16x16x32_bf16 v[56:59], v[216:219], v[36:39], v[56:59]\n"
    "v_cvt_pk_bf16_f32 v90, v76, v79\n"
    "v_mfma_f32_16x16x32_bf16 v[60:63], v[248:251], v[36:39], v[60:63]\n"
    "v_permlane16_swap_b32 v88, v89\n"
    "ds_read_b128 v[28:31], v9 offset:10240\n"
    "s_waitcnt lgkmcnt(3)\n"
    "v_mfma_f32_16x16x32_bf16 v[52:55], v[188:191], v[40:43], v[52:55]\n"
    "v_permlane16_swap_b32 v90, v91\n"
    "v_mfma_f32_16x16x32_bf16 v[56:59], v[220:223], v[40:43], v[56:59]\n"
    "s_nop 0\n"
    "v_permlane32_swap_b32 v88, v90\n"
    "v_mfma_f32_16x16x32_bf16 v[60:63], v[252:255], v[40:43], v[60:63]\n"
    "v_permlane32_swap_b32 v89, v91\n"
    "ds_read_b128 v[32:35], v9 offset:11264\n"
    "buffer_store_dwordx4 v[88:91], v10, s[36:39], s69 offen sc1\n"
    "s_add_u32 s69, s69, 0xac00\n"
    "s_barrier\n"
    "ds_read_b128 v[64:67], v8 offset:12288\n"
    "ds_read_b128 v[68:71], v8 offset:13312\n"
    "ds_read_b128 v[72:75], v8 offset:14336\n"
    "ds_read_b128 v[84:87], v11 offset:1024\n"
    "s_waitcnt lgkmcnt(0)\n"
    "v_fma_f32 v76, -v84, v12, v52\n"
    "v_fma_f32 v79, -v84, v13, v53\n"
    "v_mul_f32 v78, v76, v76\n"
    "v_mul_f32 v81, v79, v79\n"
    "v_fma_f32 v78, v78, v86, v85\n"
    "v_fma_f32 v81, v81, v86, v85\n"
    "v_mul_f32 v78, v76, v78\n"
    "v_mul_f32 v81, v79, v81\n"
    "v_exp_f32 v78, v78\n"
    "v_exp_f32 v81, v81\n"
    "v_fma_f32 v77, -v84, v14, v54\n"
    "v_fma_f32 v80, -v84, v15, v55\n"
    "v_mul_f32 v76, v76, v77\n"
    "v_mul_f32 v79, v79, v80\n"
    "v_fma_f32 v78, v78, v87, v87\n"
    "v_fma_f32 v81, v81, v87, v87\n"
    "v_rcp_f32 v78, v78\n"
    "v_rcp_f32 v81, v81\n"
    "v_mul_f32 v76, v76, v78\n"
    "v_mul_f32 v79, v79, v81\n"
    "v_cvt_pk_bf16_f32 v88, v76, v79\n"
    "v_fma_f32 v76, -v84, v16, v56\n"
    "v_fma_f32 v79, -v84, v17, v57\n"
    "v_mul_f32 v78, v76, v76\n"
    "v_mul_f32 v81, v79, v79\n"
    "v_fma_f32 v78, v78, v86, v85\n"
    "v_fma_f32 v81, v81, v86, v85\n"
    "v_mul_f32 v78, v76, v78\n"
    "v_mul_f32 v81, v79, v81\n"
    "v_exp_f32 v78, v78\n"
    "v_exp_f32 v81, v81\n"
    "v_fma_f32 v77, -v84, v18, v58\n"
    "v_fma_f32 v80, -v84, v19, v59\n"
    "v_mul_f32 v76, v76, v77\n"
    "v_mul_f32 v79, v79, v80\n"
    "v_fma_f32 v78, v78, v87, v87\n"
    "v_fma_f32 v81, v81, v87, v87\n"
    "v_rcp_f32 v78, v78\n"
    "v_rcp_f32 v81, v81\n"
    "v_mul_f32 v76, v76, v78\n"
    "v_mul_f32 v79, v79, v81\n"
    "v_cvt_pk_bf16_f32 v89, v76, v79\n"
    "v_fma_f32 v76, -v84, v20, v60\n"
    "v_mfma_f32_16x16x32_bf16 v[64:67], v[160:163], v[44:47], v[64:67]\n"
    "v_fma_f32 v79, -v84, v21, v61\n"
    "v_mfma_f32_16x16x32_bf16 v[68:71], v[192:195], v[44:47], v[68:71]\n"
    "v_mul_f32 v78, v76, v76\n"
    "v_mfma_f32_16x16x32_bf16 v[72:75], v[224:227], v[44:47], v[72:75]\n"
    "v_mul_f32 v81, v79, v79\n"
    "ds_read_b128 v[36:39], v9 offset:12288\n"
    "v_mfma_f32_16x16x32_bf16 v[64:67], v[164:167], v[48:51], v[64:67]\n"
    "v_fma_f32 v78, v78, v86, v85\n"
    "v_mfma_f32_16x16x32_bf16 v[68:71], v[196:199], v[48:51], v[68:71]\n"
    "v_fma_f32 v81, v81, v86, v85\n"
    "v_mfma_f32_16x16x32_bf16 v[72:75], v[228:231], v[48:51], v[72:75]\n"
    "v_mul_f32 v78, v76, v78\n"
    "ds_read_b128 v[40:43], v9 offset:13312\n"
    "v_mfma_f32_16x16x32_bf16 v[64:67], v[168:171], v[28:31], v[64:67]\n"
    "v_mul_f32 v81, v79, v81\n"
    "v_mfma_f32_16x16x32_bf16 v[68:71], v[200:203], v[28:31], v[68:71]\n"
    "v_exp_f32 v78, v78\n"
    "v_mfma_f32_16x16x32_bf16 v[72:75], v[232:235], v[28:31], v[72:75]\n"
    "v_exp_f32 v81, v81\n"
    "ds_read_b128 v[44:47], v9 offset:14336\n"
    "v_mfma_f32_16x16x32_bf16 v[64:67], v[172:175], v[32:35], v[64:67]\n"
    "v_fma_f32 v77, -v84, v22, v62\n"
    "v_mfma_f32_16x16x32_bf16 v[68:71], v[204:207], v[32:35], v[68:71]\n"
    "v_fma_f32 v80, -v84, v23, v63\n"
    "v_mfma_f32_16x16x32_bf16 v[72:75], v[236:239], v[32:35], v[72:75]\n"
    "v_mul_f32 v76, v76, v77\n"
    "ds_read_b128 v[48:51], v9 offset:15360\n"
    "s_waitcnt lgkmcnt(3)\n"
    "v_mfma_f32_16x16x32_bf16 v[64:67], v[176:179], v[36:39], v[64:67]\n"
    "v_mul_f32 v79, v79, v80\n"
    "v_mfma_f32_16x16x32_bf16 v[68:71], v[208:211], v[36:39], v[68:71]\n"
    "v_fma_f32 v78, v78, v87, v87\n"
    "v_mfma_f32_16x16x32_bf16 v[72:75], v[240:243], v[36:39], v[72:75]\n"
    "v_fma_f32 v81, v81, v87, v87\n"
    "ds_read_b128 v[28:31], v9 offset:16384\n"
    "s_waitcnt lgkmcnt(3)\n"
    "v_mfma_f32_16x16x32_bf16 v[64:67], v[180:183], v[40:43], v[64:67]\n"
    "v_rcp_f32 v78, v78\n"
    "v_mfma_f32_16x16x32_bf16 v[68:71], v[212:215], v[40:43], v[68:71]\n"
    "v_rcp_f32 v81, v81\n"
    "v_mfma_f32_16x16x32_bf16 v[72:75], v[244:247], v[40:43], v[72:75]\n"
    "v_mul_f32 v76, v76, v78\n"
    "ds_read_b128 v[32:35], v9 offset:17408\n"
    "s_waitcnt lgkmcnt(3)\n"
    "v_mfma_f32_16x16x32_bf16 v[64:67], v[184:187], v[44:47], v[64:67]\n"
    "v_mul_f32 v79, v79, v81\n"
    "v_mfma_f32_16x16x32_bf16 v[68:71], v[216:219], v[44:47], v[68:71]\n"
    "v_cvt_pk_bf16_f32 v90, v76, v79\n"
    "v_mfma_f32_16x16x32_bf16 v[72:75], v[248:251], v[44:47], v[72:75]\n"
    "v_permlane16_swap_b32 v88, v89\n"
    "ds_read_b128 v[36:39], v9 offset:18432\n"
    "s_waitcnt lgkmcnt(3)\n"
    "v_mfma_f32_16x16x32_bf16 v[64:67], v[188:191], v[48:51], v[64:67]\n"
    "v_permlane16_swap_b32 v90, v91\n"
    "v_mfma_f32_16x16x32_bf16 v[68:71], v[220:223], v[48:51], v[68:71]\n"
    "s_nop 0\n"
    "v_permlane32_swap_b32 v88, v90\n"
    "v_mfma_f32_16x16x32_bf16 v[72:75], v[252:255], v[48:51], v[72:75]\n"
    "v_permlane32_swap_b32 v89, v91\n"
    "ds_read_b128 v[40:43], v9 offset:19456\n"
    "buffer_store_dwordx4 v[88:91], v10, s[36:39], s69 offen sc1\n"
    "s_add_u32 s69, s69, 0xac00\n"
    "s_barrier\n"
    "ds_read_b128 v[52:55], v8\n"
    "ds_read_b128 v[56:59], v8 offset:1024\n"
    "ds_read_b128 v[60:63], v8 offset:2048\n"
    "ds_read_b128 v[124:127], v11 offset:1280\n"
    "s_waitcnt lgkmcnt(0)\n"
    "v_fma_f32 v76, -v124, v12, v64\n"
    "v_fma_f32 v79, -v124, v13, v65\n"
    "v_mul_f32 v78, v76, v76\n"
    "v_mul_f32 v81, v79, v79\n"
    "v_fma_f32 v78, v78, v126, v125\n"
    "v_fma_f32 v81, v81, v126, v125\n"
    "v_mul_f32 v78, v76, v78\n"
    "v_mul_f32 v81, v79, v81\n"
    "v_exp_f32 v78, v78\n"
    "v_exp_f32 v81, v81\n"
    "v_fma_f32 v77, -v124, v14, v66\n"
    "v_fma_f32 v80, -v124, v15, v67\n"
    "v_mul_f32 v76, v76, v77\n"
    "v_mul_f32 v79, v79, v80\n"
    "v_fma_f32 v78, v78, v127, v127\n"
    "v_fma_f32 v81, v81, v127, v127\n"
    "v_rcp_f32 v78, v78\n"
    "v_rcp_f32 v81, v81\n"
    "v_mul_f32 v76, v76, v78\n"
    "v_mul_f32 v79, v79, v81\n"
    "v_cvt_pk_bf16_f32 v88, v76, v79\n"
    "v_fma_f32 v76, -v124, v16, v68\n"
    "v_fma_f32 v79, -v124, v17, v69\n"
    "v_mul_f32 v78, v76, v76\n"
    "v_mul_f32 v81, v79, v79\n"
    "v_fma_f32 v78, v78, v126, v125\n"
    "v_fma_f32 v81, v81, v126, v125\n"
    "v_mul_f32 v78, v76, v78\n"
    "v_mul_f32 v81, v79, v81\n"
    "v_exp_f32 v78, v78\n"
    "v_exp_f32 v81, v81\n"
    "v_fma_f32 v77, -v124, v18, v70\n"
    "v_fma_f32 v80, -v124, v19, v71\n"
    "v_mul_f32 v76, v76, v77\n"
    "v_mul_f32 v79, v79, v80\n"
    "v_fma_f32 v78, v78, v127, v127\n"
    "v_fma_f32 v81, v81, v127, v127\n"
    "v_rcp_f32 v78, v78\n"
    "v_rcp_f32 v81, v81\n"
    "v_mul_f32 v76, v76, v78\n"
    "v_mul_f32 v79, v79, v81\n"
    "v_cvt_pk_bf16_f32 v89, v76, v79\n"
    "v_fma_f32 v76, -v124, v20, v72\n"
    "v_mfma_f32_16x16x32_bf16 v[52:55], v[160:163], v[28:31], v[52:55]\n"
    "v_fma_f32 v79, -v124, v21, v73\n"
    "v_mfma_f32_16x16x32_bf16 v[56:59], v[192:195], v[28:31], v[56:59]\n"
    "v_mul_f32 v78, v76, v76\n"
    "v_mfma_f32_16x16x32_bf16 v[60:63], v[224:227], v[28:31], v[60:63]\n"
    "v_mul_f32 v81, v79, v79\n"
    "ds_read_b128 v[44:47], v9 offset:20480\n"
    "v_mfma_f32_16x16x32_bf16 v[52:55], v[164:167], v[32:35], v[52:55]\n"
    "v_fma_f32 v78, v78, v126, v125\n"
    "v_mfma_f32_16x16x32_bf16 v[56:59], v[196:199], v[32:35], v[56:59]\n"
    "v_fma_f32 v81, v81, v126, v125\n"
    "v_mfma_f32_16x16x32_bf16 v[60:63], v[228:231], v[32:35], v[60:63]\n"
    "v_mul_f32 v78, v76, v78\n"
    "ds_read_b128 v[48:51], v9 offset:21504\n"
    "v_mfma_f32_16x16x32_bf16 v[52:55], v[168:171], v[36:39], v[52:55]\n"
    "v_mul_f32 v81, v79, v81\n"
    "v_mfma_f32_16x16x32_bf16 v[56:59], v[200:203], v[36:39], v[56:59]\n"
    "v_exp_f32 v78, v78\n"
    "v_mfma_f32_16x16x32_bf16 v[60:63], v[232:235], v[36:39], v[60:63]\n"
    "v_exp_f32 v81, v81\n"
    "ds_read_b128 v[28:31], v9 offset:22528\n"
    "v_mfma_f32_16x16x32_bf16 v[52:55], v[172:175], v[40:43], v[52:55]\n"
    "v_fma_f32 v77, -v124, v22, v74\n"
    "v_mfma_f32_16x16x32_bf16 v[56:59], v[204:207], v[40:43], v[56:59]\n"
    "v_fma_f32 v80, -v124, v23, v75\n"
    "v_mfma_f32_16x16x32_bf16 v[60:63], v[236:239], v[40:43], v[60:63]\n"
    "v_mul_f32 v76, v76, v77\n"
    "ds_read_b128 v[32:35], v9 offset:23552\n"
    "s_waitcnt lgkmcnt(3)\n"
    "v_mfma_f32_16x16x32_bf16 v[52:55], v[176:179], v[44:47], v[52:55]\n"
    "v_mul_f32 v79, v79, v80\n"
    "v_mfma_f32_16x16x32_bf16 v[56:59], v[208:211], v[44:47], v[56:59]\n"
    "v_fma_f32 v78, v78, v127, v127\n"
    "v_mfma_f32_16x16x32_bf16 v[60:63], v[240:243], v[44:47], v[60:63]\n"
    "v_fma_f32 v81, v81, v127, v127\n"
    "ds_read_b128 v[36:39], v9 offset:24576\n"
    "s_waitcnt lgkmcnt(3)\n"
    "v_mfma_f32_16x16x32_bf16 v[52:55], v[180:183], v[48:51], v[52:55]\n"
    "v_rcp_f32 v78, v78\n"
    "v_mfma_f32_16x16x32_bf16 v[56:59], v[212:215], v[48:51], v[56:59]\n"
    "v_rcp_f32 v81, v81\n"
    "v_mfma_f32_16x16x32_bf16 v[60:63], v[244:247], v[48:51], v[60:63]\n"
    "v_mul_f32 v76, v76, v78\n"
    "ds_read_b128 v[40:43], v9 offset:25600\n"
    "s_waitcnt lgkmcnt(3)\n"
    "v_mfma_f32_16x16x32_bf16 v[52:55], v[184:187], v[28:31], v[52:55]\n"
    "v_mul_f32 v79, v79, v81\n"
    "v_mfma_f32_16x16x32_bf16 v[56:59], v[216:219], v[28:31], v[56:59]\n"
    "v_cvt_pk_bf16_f32 v90, v76, v79\n"
    "v_mfma_f32_16x16x32_bf16 v[60:63], v[248:251], v[28:31], v[60:63]\n"
    "v_permlane16_swap_b32 v88, v89\n"
    "ds_read_b128 v[44:47], v9 offset:26624\n"
    "s_waitcnt lgkmcnt(3)\n"
    "v_mfma_f32_16x16x32_bf16 v[52:55], v[188:191], v[32:35], v[52:55]\n"
    "v_permlane16_swap_b32 v90, v91\n"
    "v_mfma_f32_16x16x32_bf16 v[56:59], v[220:223], v[32:35], v[56:59]\n"
    "s_nop 0\n"
    "v_permlane32_swap_b32 v88, v90\n"
    "v_mfma_f32_16x16x32_bf16 v[60:63], v[252:255], v[32:35], v[60:63]\n"
    "v_permlane32_swap_b32 v89, v91\n"
    "ds_read_b128 v[48:51], v9 offset:27648\n"
    "buffer_store_dwordx4 v[88:91], v10, s[36:39], s69 offen sc1\n"
    "s_add_u32 s69, s69, 0xac00\n"
    "s_barrier\n"
    "ds_read_b128 v[64:67], v8 offset:12288\n"
    "ds_read_b128 v[68:71], v8 offset:13312\n"
    "ds_read_b128 v[72:75], v8 offset:14336\n"
    "ds_read_b128 v[84:87], v11 offset:1536\n"
    "s_waitcnt lgkmcnt(0)\n"
    "v_fma_f32 v76, -v84, v12, v52\n"
    "v_fma_f32 v79, -v84, v13, v53\n"
    "v_mul_f32 v78, v76, v76\n"
    "v_mul_f32 v81, v79, v79\n"
    "v_fma_f32 v78, v78, v86, v85\n"
    "v_fma_f32 v81, v81, v86, v85\n"
    "v_mul_f32 v78, v76, v78\n"
    "v_mul_f32 v81, v79, v81\n"
    "v_exp_f32 v78, v78\n"
    "v_exp_f32 v81, v81\n"
    "v_fma_f32 v77, -v84, v14, v54\n"
    "v_fma_f32 v80, -v84, v15, v55\n"
    "v_mul_f32 v76, v76, v77\n"
    "v_mul_f32 v79, v79, v80\n"
    "v_fma_f32 v78, v78, v87, v87\n"
    "v_fma_f32 v81, v81, v87, v87\n"
    "v_rcp_f32 v78, v78\n"
    "v_rcp_f32 v81, v81\n"
    "v_mul_f32 v76, v76, v78\n"
    "v_mul_f32 v79, v79, v81\n"
    "v_cvt_pk_bf16_f32 v88, v76, v79\n"
    "v_fma_f32 v76, -v84, v16, v56\n"
    "v_fma_f32 v79, -v84, v17, v57\n"
    "v_mul_f32 v78, v76, v76\n"
    "v_mul_f32 v81, v79, v79\n"
    "v_fma_f32 v78, v78, v86, v85\n"
    "v_fma_f32 v81, v81, v86, v85\n"
    "v_mul_f32 v78, v76, v78\n"
    "v_mul_f32 v81, v79, v81\n"
    "v_exp_f32 v78, v78\n"
    "v_exp_f32 v81, v81\n"
    "v_fma_f32 v77, -v84, v18, v58\n"
    "v_fma_f32 v80, -v84, v19, v59\n"
    "v_mul_f32 v76, v76, v77\n"
    "v_mul_f32 v79, v79, v80\n"
    "v_fma_f32 v78, v78, v87, v87\n"
    "v_fma_f32 v81, v81, v87, v87\n"
    "v_rcp_f32 v78, v78\n"
    "v_rcp_f32 v81, v81\n"
    "v_mul_f32 v76, v76, v78\n"
    "v_mul_f32 v79, v79, v81\n"
    "v_cvt_pk_bf16_f32 v89, v76, v79\n"
    "v_fma_f32 v76, -v84, v20, v60\n"
    "v_mfma_f32_16x16x32_bf16 v[64:67], v[160:163], v[36:39], v[64:67]\n"
    "v_fma_f32 v79, -v84, v21, v61\n"
    "v_mfma_f32_16x16x32_bf16 v[68:71], v[192:195], v[36:39], v[68:71]\n"
    "v_mul_f32 v78, v76, v76\n"
    "v_mfma_f32_16x16x32_bf16 v[72:75], v[224:227], v[36:39], v[72:75]\n"
    "v_mul_f32 v81, v79, v79\n"
    "ds_read_b128 v[28:31], v9 offset:28672\n"
    "v_mfma_f32_16x16x32_bf16 v[64:67], v[164:167], v[40:43], v[64:67]\n"
    "v_fma_f32 v78, v78, v86, v85\n"
    "v_mfma_f32_16x16x32_bf16 v[68:71], v[196:199], v[40:43], v[68:71]\n"
    "v_fma_f32 v81, v81, v86, v85\n"
    "v_mfma_f32_16x16x32_bf16 v[72:75], v[228:231], v[40:43], v[72:75]\n"
    "v_mul_f32 v78, v76, v78\n"
    "ds_read_b128 v[32:35], v9 offset:29696\n"
    "v_mfma_f32_16x16x32_bf16 v[64:67], v[168:171], v[44:47], v[64:67]\n"
    "v_mul_f32 v81, v79, v81\n"
    "v_mfma_f32_16x16x32_bf16 v[68:71], v[200:203], v[44:47], v[68:71]\n"
    "v_exp_f32 v78, v78\n"
    "v_mfma_f32_16x16x32_bf16 v[72:75], v[232:235], v[44:47], v[72:75]\n"
    "v_exp_f32 v81, v81\n"
    "ds_read_b128 v[36:39], v9 offset:30720\n"
    "v_mfma_f32_16x16x32_bf16 v[64:67], v[172:175], v[48:51], v[64:67]\n"
    "v_fma_f32 v77, -v84, v22, v62\n"
    "v_mfma_f32_16x16x32_bf16 v[68:71], v[204:207], v[48:51], v[68:71]\n"
    "v_fma_f32 v80, -v84, v23, v63\n"
    "v_mfma_f32_16x16x32_bf16 v[72:75], v[236:239], v[48:51], v[72:75]\n"
    "v_mul_f32 v76, v76, v77\n"
    "ds_read_b128 v[40:43], v9 offset:31744\n"
    "s_waitcnt lgkmcnt(3)\n"
    "v_mfma_f32_16x16x32_bf16 v[64:67], v[176:179], v[28:31], v[64:67]\n"
    "v_mul_f32 v79, v79, v80\n"
    "v_mfma_f32_16x16x32_bf16 v[68:71], v[208:211], v[28:31], v[68:71]\n"
    "v_fma_f32 v78, v78, v87, v87\n"
    "v_mfma_f32_16x16x32_bf16 v[72:75], v[240:243], v[28:31], v[72:75]\n"
    "v_fma_f32 v81, v81, v87, v87\n"
    "s_waitcnt lgkmcnt(2)\n"
    "v_mfma_f32_16x16x32_bf16 v[64:67], v[180:183], v[32:35], v[64:67]\n"
    "v_rcp_f32 v78, v78\n"
    "v_mfma_f32_16x16x32_bf16 v[68:71], v[212:215], v[32:35], v[68:71]\n"
    "v_rcp_f32 v81, v81\n"
    "v_mfma_f32_16x16x32_bf16 v[72:75], v[244:247], v[32:35], v[72:75]\n"
    "v_mul_f32 v76, v76, v78\n"
    "s_waitcnt lgkmcnt(1)\n"
    "v_mfma_f32_16x16x32_bf16 v[64:67], v[184:187], v[36:39], v[64:67]\n"
    "v_mul_f32 v79, v79, v81\n"
    "v_mfma_f32_16x16x32_bf16 v[68:71], v[216:219], v[36:39], v[68:71]\n"
    "v_cvt_pk_bf16_f32 v90, v76, v79\n"
    "v_mfma_f32_16x16x32_bf16 v[72:75], v[248:251], v[36:39], v[72:75]\n"
    "v_permlane16_swap_b32 v88, v89\n"
    "s_waitcnt lgkmcnt(0)\n"
    "v_mfma_f32_16x16x32_bf16 v[64:67], v[188:191], v[40:43], v[64:67]\n"
    "v_permlane16_swap_b32 v90, v91\n"
    "v_mfma_f32_16x16x32_bf16 v[68:71], v[220:223], v[40:43], v[68:71]\n"
    "s_nop 0\n"
    "v_permlane32_swap_b32 v88, v90\n"
    "v_mfma_f32_16x16x32_bf16 v[72:75], v[252:255], v[40:43], v[72:75]\n"
    "v_permlane32_swap_b32 v89, v91\n"
    "buffer_store_dwordx4 v[88:91], v10, s[36:39], s69 offen sc1\n"
    "s_add_u32 s69, s69, 0xac00\n"
    "ds_read_b128 v[124:127], v11 offset:1792\n"
    "s_waitcnt lgkmcnt(0)\n"
    "s_barrier\n"
    "v_fma_f32 v76, -v124, v12, v64\n"
    "v_fma_f32 v79, -v124, v13, v65\n"
    "v_mul_f32 v78, v76, v76\n"
    "v_mul_f32 v81, v79, v79\n"
    "v_fma_f32 v78, v78, v126, v125\n"
    "v_fma_f32 v81, v81, v126, v125\n"
    "v_mul_f32 v78, v76, v78\n"
    "v_mul_f32 v81, v79, v81\n"
    "v_exp_f32 v78, v78\n"
    "v_exp_f32 v81, v81\n"
    "v_fma_f32 v77, -v124, v14, v66\n"
    "v_fma_f32 v80, -v124, v15, v67\n"
    "v_mul_f32 v76, v76, v77\n"
    "v_mul_f32 v79, v79, v80\n"
    "v_fma_f32 v78, v78, v127, v127\n"
    "v_fma_f32 v81, v81, v127, v127\n"
    "v_rcp_f32 v78, v78\n"
    "v_rcp_f32 v81, v81\n"
    "v_mul_f32 v76, v76, v78\n"
    "v_mul_f32 v79, v79, v81\n"
    "v_cvt_pk_bf16_f32 v88, v76, v79\n"
    "v_fma_f32 v76, -v124, v16, v68\n"
    "v_fma_f32 v79, -v124, v17, v69\n"
    "v_mul_f32 v78, v76, v76\n"
    "v_mul_f32 v81, v79, v79\n"
    "v_fma_f32 v78, v78, v126, v125\n"
    "v_fma_f32 v81, v81, v126, v125\n"
    "v_mul_f32 v78, v76, v78\n"
    "v_mul_f32 v81, v79, v81\n"
    "v_exp_f32 v78, v78\n"
    "v_exp_f32 v81, v81\n"
    "v_fma_f32 v77, -v124, v18, v70\n"
    "v_fma_f32 v80, -v124, v19, v71\n"
    "v_mul_f32 v76, v76, v77\n"
    "v_mul_f32 v79, v79, v80\n"
    "v_fma_f32 v78, v78, v127, v127\n"
    "v_fma_f32 v81, v81, v127, v127\n"
    "v_rcp_f32 v78, v78\n"
    "v_rcp_f32 v81, v81\n"
    "v_mul_f32 v76, v76, v78\n"
    "v_mul_f32 v79, v79, v81\n"
    "v_cvt_pk_bf16_f32 v89, v76, v79\n"
    "v_fma_f32 v76, -v124, v20, v72\n"
    "v_fma_f32 v79, -v124, v21, v73\n"
    "v_mul_f32 v78, v76, v76\n"
    "v_mul_f32 v81, v79, v79\n"
    "v_fma_f32 v78, v78, v126, v125\n"
    "v_fma_f32 v81, v81, v126, v125\n"
    "v_mul_f32 v78, v76, v78\n"
    "v_mul_f32 v81, v79, v81\n"
    "v_exp_f32 v78, v78\n"
    "v_exp_f32 v81, v81\n"
    "v_fma_f32 v77, -v124, v22, v74\n"
    "v_fma_f32 v80, -v124, v23, v75\n"
    "v_mul_f32 v76, v76, v77\n"
    "v_mul_f32 v79, v79, v80\n"
    "v_fma_f32 v78, v78, v127, v127\n"
    "v_fma_f32 v81, v81, v127, v127\n"
    "v_rcp_f32 v78, v78\n"
    "v_rcp_f32 v81, v81\n"
    "v_mul_f32 v76, v76, v78\n"
    "v_mul_f32 v79, v79, v81\n"
    "v_cvt_pk_bf16_f32 v90, v76, v79\n"
    "v_permlane16_swap_b32 v88, v89\n"
    "s_nop 0\n"
    "v_permlane16_swap_b32 v90, v91\n"
    "s_nop 1\n"
    "v_permlane32_swap_b32 v88, v90\n"
    "v_permlane32_swap_b32 v89, v91\n"
    "buffer_store_dwordx4 v[88:91], v10, s[36:39], s69 offen sc1\n"
    "s_add_u32 s69, s69, 0xac00\n"
    ".Lp3_join:\n"
    "s_mul_i32 s60, s57, 0x9000\n"
    "s_add_u32 s60, s60, 0x14800\n"
    "s_lshl_b32 s61, s57, 11\n"
    "s_add_u32 s62, s60, s61\n"
    "v_add_u32 v8, s62, v1\n"
    "s_xor_b32 s61, s61, 0x800\n"
    "s_add_u32 s62, s60, s61\n"
    "v_add_u32 v9, s62, v1\n"
    "s_lshl_b32 s60, s53, 11\n"
    "s_add_u32 s60, s60, 0x17800\n"
    "s_mul_i32 s61, s57, 0x9000\n"
    "s_add_u32 s62, s60, s61\n"
    "v_add_u32 v11, s62, v1\n"
    "s_sub_u32 s61, 0x9000, s61\n"
    "s_add_u32 s62, s60, s61\n"
    "v_add_u32 v10, s62, v1\n"
    "ds_read_b128 v[160:163], v8\n"
    "ds_read_b128 v[164:167], v8 offset:1024\n"
    "ds_read_b128 v[168:171], v9\n"
    "ds_read_b128 v[172:175], v9 offset:1024\n"
    "ds_read_b128 v[176:179], v8 offset:4096\n"
    "ds_read_b128 v[180:183], v8 offset:5120\n"
    "ds_read_b128 v[184:187], v9 offset:4096\n"
    "ds_read_b128 v[188:191], v9 offset:5120\n"
    "ds_read_b128 v[192:195], v8 offset:8192\n"
    "ds_read_b128 v[196:199], v8 offset:9216\n"
    "ds_read_b128 v[200:203], v9 offset:8192\n"
    "ds_read_b128 v[204:207], v9 offset:9216\n"
    "ds_read_b128 v[208:211], v8 offset:12288\n"
    "ds_read_b128 v[212:215], v8 offset:13312\n"
    "ds_read_b128 v[216:219], v9 offset:12288\n"
    "ds_read_b128 v[220:223], v9 offset:13312\n"
    "ds_read_b128 v[224:227], v8 offset:16384\n"
    "ds_read_b128 v[228:231], v8 offset:17408\n"
    "ds_read_b128 v[232:235], v9 offset:16384\n"
    "ds_read_b128 v[236:239], v9 offset:17408\n"
    "s_mul_i32 s60, s57, 0x1000\n"
    "s_sub_u32 s60, s60, 0x14800\n"
    "v_add_u32 v60, s60, v8\n"
    "v_add_u32 v61, s60, v9\n"
    "s_mul_i32 s60, s57, 0xa000\n"
    "s_add_u32 s34, s33, s60\n"
    "s_add_u32 s64, s71, 0x9000\n"
    "s_add_u32 m0, s34, 0\n"
    "s_nop 0\n"
    "buffer_load_dwordx4 v1, s[36:39], s64 offen sc1 lds\n"
    "s_add_u32 s64, s71, 0xa000\n"
    "s_add_u32 m0, s34, 0x1000\n"
    "s_nop 0\n"
    "buffer_load_dwordx4 v1, s[36:39], s64 offen sc1 lds\n"
    "s_add_u32 s64, s71, 0xb000\n"
    "s_add_u32 m0, s34, 0x2000\n"
    "s_nop 0\n"
    "buffer_load_dwordx4 v1, s[36:39], s64 offen sc1 lds\n"
    "s_add_u32 s64, s71, 0xc000\n"
    "s_add_u32 m0, s34, 0x3000\n"
    "s_nop 0\n"
    "buffer_load_dwordx4 v1, s[36:39], s64 offen sc1 lds\n"
    "s_add_u32 s64, s71, 0xd000\n"
    "s_add_u32 m0, s34, 0x4000\n"
    "s_nop 0\n"
    "buffer_load_dwordx4 v1, s[36:39], s64 offen sc1 lds\n"
    "s_add_u32 s64, s71, 0xe000\n"
    "s_add_u32 m0, s34, 0x5000\n"
    "s_nop 0\n"
    "buffer_load_dwordx4 v1, s[36:39], s64 offen sc1 lds\n"
    "s_add_u32 s64, s71, 0xf000\n"
    "s_add_u32 m0, s34, 0x6000\n"
    "s_nop 0\n"
    "buffer_load_dwordx4 v1, s[36:39], s64 offen sc1 lds\n"
    "s_add_u32 s64, s71, 0x10000\n"
    "s_add_u32 m0, s34, 0x7000\n"
    "s_nop 0\n"
    "buffer_load_dwordx4 v1, s[36:39], s64 offen sc1 lds\n"
    "s_add_u32 s64, s71, 0x11000\n"
    "s_add_u32 m0, s34, 0x8000\n"
    "s_nop 0\n"
    "buffer_load_dwordx4 v1, s[36:39], s64 offen sc1 lds\n"
    "s_add_u32 s64, s71, 0x12000\n"
    "s_add_u32 m0, s34, 0x9000\n"
    "s_nop 0\n"
    "buffer_load_dwordx4 v1, s[36:39], s64 offen sc1 lds\n"
    "s_lshl_b32 s69, s50, 2\n"
    "s_add_u32 s69, s69, s53\n"
    "s_mul_i32 s69, s69, 0xac00\n"
    "s_mul_i32 s60, s57, 0x5800\n"
    "s_add_u32 s69, s69, s60\n"
    "s_add_u32 s69, s69, 0x888000\n"
    "s_mov_b32 s76, 0\n"
    "v_lshlrev_b32 v62, 10, v3\n"
    "v_lshl_add_u32 v62, v2, 4, v62\n"
    "s_waitcnt lgkmcnt(0)\n"
    "s_cmp_eq_u32 s57, 0\n"
    "s_nop 0\n"
    "s_cbranch_scc1 .Lp4_b\n"
    "s_waitcnt vmcnt(10)\n"
    ".Lp4_b:\n"
    "s_barrier\n"
    "s_add_u32 s65, s69, 0\n"
    "buffer_load_dwordx4 v[64:67], v1, s[36:39], s65 offen sc1\n"
    "buffer_load_dwordx4 v[68:71], v1, s[36:39], s65 offen offset:1024 sc1\n"
    "buffer_load_dwordx4 v[72:75], v1, s[36:39], s65 offen offset:2048 sc1\n"
    "buffer_load_dwordx4 v[76:79], v1, s[36:39], s65 offen offset:3072 sc1\n"
    "s_add_u32 s65, s69, 0x1000\n"
    "buffer_load_dwordx4 v[80:83], v1, s[36:39], s65 offen sc1\n"
    "buffer_load_dwordx4 v[84:87], v1, s[36:39], s65 offen offset:1024 sc1\n"
    "buffer_load_dwordx4 v[88:91], v1, s[36:39], s65 offen offset:2048 sc1\n"
    "buffer_load_dwordx4 v[92:95], v1, s[36:39], s65 offen offset:3072 sc1\n"
    "v_lshlrev_b32 v12, 16, v4\n"
    "v_and_b32 v13, 0xffff0000, v4\n"
    "v_lshlrev_b32 v14, 16, v5\n"
    "v_and_b32 v15, 0xffff0000, v5\n"
    "v_lshlrev_b32 v16, 16, v6\n"
    "v_and_b32 v17, 0xffff0000, v6\n"
    "v_lshlrev_b32 v18, 16, v7\n"
    "v_and_b32 v19, 0xffff0000, v7\n"
    "s_waitcnt vmcnt(7)\n"
    "v_mfma_f32_16x16x32_bf16 v[12:15], v[160:163], v[64:67], v[12:15]\n"
    "s_add_u32 s64, s71, 0x13000\n"
    "s_add_u32 m0, s35, 0x14800\n"
    "v_mfma_f32_16x16x32_bf16 v[16:19], v[164:167], v[64:67], v[16:19]\n"
    "v_mfma_f32_16x16x32_bf16 v[20:23], v[168:171], v[64:67], 0\n"
    "buffer_load_dwordx4 v1, s[36:39], s64 offen sc1 lds\n"
    "s_add_u32 s64, s71, 0x14000\n"
    "v_mfma_f32_16x16x32_bf16 v[24:27], v[172:175], v[64:67], 0\n"
    "s_waitcnt vmcnt(7)\n"
    "v_mfma_f32_16x16x32_bf16 v[12:15], v[176:179], v[68:71], v[12:15]\n"
    "s_add_u32 m0, s35, 0x15800\n"
    "s_nop 0\n"
    "buffer_load_dwordx4 v1, s[36:39], s64 offen sc1 lds\n"
    "v_mfma_f32_16x16x32_bf16 v[16:19], v[180:183], v[68:71], v[16:19]\n"
    "v_mfma_f32_16x16x32_bf16 v[20:23], v[184:187], v[68:71], v[20:23]\n"
    "s_add_u32 s64, s71, 0x15000\n"
    "s_cmp_eq_u32 s57, 1\n"
    "s_cselect_b32 s64, 0x4000000, s64\n"
    "v_mfma_f32_16x16x32_bf16 v[24:27], v[188:191], v[68:71], v[24:27]\n"
    "s_waitcnt vmcnt(7)\n"
    "v_mfma_f32_16x16x32_bf16 v[12:15], v[192:195], v[72:75], v[12:15]\n"
    "s_add_u32 m0, s35, 0x16800\n"
    "s_nop 0\n"
    "buffer_load_dwordx4 v1, s[36:39], s64 offen sc1 lds\n"
    "v_mfma_f32_16x16x32_bf16 v[16:19], v[196:199], v[72:75], v[16:19]\n"
    "v_mfma_f32_16x16x32_bf16 v[20:23], v[200:203], v[72:75], v[20:23]\n"
    "s_add_u32 s65, s69, 0x2000\n"
    "buffer_load_dwordx4 v[96:99], v1, s[36:39], s65 offen sc1\n"
    "v_mfma_f32_16x16x32_bf16 v[24:27], v[204:207], v[72:75], v[24:27]\n"
    "s_waitcnt vmcnt(8)\n"
    "v_mfma_f32_16x16x32_bf16 v[12:15], v[208:211], v[76:79], v[12:15]\n"
    "buffer_load_dwordx4 v[100:103], v1, s[36:39], s65 offen offset:1024 sc1\n"
    "buffer_load_dwordx4 v[104:107], v1, s[36:39], s65 offen offset:2048 sc1\n"
    "v_mfma_f32_16x16x32_bf16 v[16:19], v[212:215], v[76:79], v[16:19]\n"
    "v_mfma_f32_16x16x32_bf16 v[20:23], v[216:219], v[76:79], v[20:23]\n"
    "buffer_load_dwordx4 v[108:111], v1, s[36:39], s65 offen offset:3072 sc1\n"
    "s_add_u32 s65, s69, 0x3000\n"
    "ds_read_b128 v[44:47], v8 offset:20480\n"
    "v_mfma_f32_16x16x32_bf16 v[24:27], v[220:223], v[76:79], v[24:27]\n"
    "ds_read_b128 v[48:51], v8 offset:21504\n"
    "s_waitcnt vmcnt(10)\n"
    "v_mfma_f32_16x16x32_bf16 v[12:15], v[224:227], v[80:83], v[12:15]\n"
    "buffer_load_dwordx4 v[112:115], v1, s[36:39], s65 offen sc1\n"
    "buffer_load_dwordx4 v[116:119], v1, s[36:39], s65 offen offset:1024 sc1\n"
    "ds_read_b128 v[52:55], v9 offset:20480\n"
    "v_mfma_f32_16x16x32_bf16 v[16:19], v[228:231], v[80:83], v[16:19]\n"
    "ds_read_b128 v[56:59], v9 offset:21504\n"
    "v_mfma_f32_16x16x32_bf16 v[20:23], v[232:235], v[80:83], v[20:23]\n"
    "buffer_load_dwordx4 v[120:123], v1, s[36:39], s65 offen offset:2048 sc1\n"
    "buffer_load_dwordx4 v[124:127], v1, s[36:39], s65 offen offset:3072 sc1\n"
    "ds_read_b128 v[28:31], v8 offset:24576\n"
    "v_mfma_f32_16x16x32_bf16 v[24:27], v[236:239], v[80:83], v[24:27]\n"
    "ds_read_b128 v[32:35], v8 offset:25600\n"
    "s_waitcnt vmcnt(13) lgkmcnt(5)\n"
    "v_mfma_f32_16x16x32_bf16 v[12:15], v[44:47], v[84:87], v[12:15]\n"
    "s_add_u32 s65, s69, 0x4000\n"
    "buffer_load_dwordx4 v[128:131], v1, s[36:39], s65 offen sc1\n"
    "ds_read_b128 v[36:39], v9 offset:24576\n"
    "s_waitcnt lgkmcnt(5)\n"
    "v_mfma_f32_16x16x32_bf16 v[16:19], v[48:51], v[84:87], v[16:19]\n"
    "ds_read_b128 v[40:43], v9 offset:25600\n"
    "s_waitcnt lgkmcnt(5)\n"
    "v_mfma_f32_16x16x32_bf16 v[20:23], v[52:55], v[84:87], v[20:23]\n"
    "buffer_load_dwordx4 v[132:135], v1, s[36:39], s65 offen offset:1024 sc1\n"
    "buffer_load_dwordx4 v[136:139], v1, s[36:39], s65 offen offset:2048 sc1\n"
    "ds_read_b128 v[44:47], v8 offset:28672\n"
    "s_waitcnt lgkmcnt(5)\n"
    "v_mfma_f32_16x16x32_bf16 v[24:27], v[56:59], v[84:87], v[24:27]\n"
    "ds_read_b128 v[48:51], v8 offset:29696\n"
    "s_waitcnt vmcnt(15) lgkmcnt(5)\n"
    "v_mfma_f32_16x16x32_bf16 v[12:15], v[28:31], v[88:91], v[12:15]\n"
    "buffer_load_dwordx4 v[140:143], v1, s[36:39], s65 offen offset:3072 sc1\n"
    "s_add_u32 s65, s69, 0x5000\n"
    "ds_read_b128 v[52:55], v9 offset:28672\n"
    "s_waitcnt lgkmcnt(5)\n"
    "v_mfma_f32_16x16x32_bf16 v[16:19], v[32:35], v[88:91], v[16:19]\n"
    "ds_read_b128 v[56:59], v9 offset:29696\n"
    "s_waitcnt lgkmcnt(5)\n"
    "v_mfma_f32_16x16x32_bf16 v[20:23], v[36:39], v[88:91], v[20:23]\n"
    "buffer_load_dwordx4 v[144:147], v1, s[36:39], s65 offen sc1\n"
    "s_cmp_eq_u32 s57, 1\n"
    "s_cselect_b32 s65, 0x4000000, s65\n"
    "ds_read_b128 v[28:31], v8 offset:32768\n"
    "s_waitcnt lgkmcnt(5)\n"
    "v_mfma_f32_16x16x32_bf16 v[24:27], v[40:43], v[88:91], v[24:27]\n"
    "ds_read_b128 v[32:35], v8 offset:33792\n"
    "s_waitcnt vmcnt(16) lgkmcnt(5)\n"
    "v_mfma_f32_16x16x32_bf16 v[12:15], v[44:47], v[92:95], v[12:15]\n"
    "buffer_load_dwordx4 v[148:151], v1, s[36:39], s65 offen offset:1024 sc1\n"
    "ds_read_b128 v[36:39], v9 offset:32768\n"
    "s_waitcnt lgkmcnt(5)\n"
    "v_mfma_f32_16x16x32_bf16 v[16:19], v[48:51], v[92:95], v[16:19]\n"
    "ds_read_b128 v[40:43], v9 offset:33792\n"
    "s_waitcnt lgkmcnt(5)\n"
    "v_mfma_f32_16x16x32_bf16 v[20:23], v[52:55], v[92:95], v[20:23]\n"
    "s_barrier\n"
    "ds_read_b128 v[44:47], v60\n"
    "s_waitcnt lgkmcnt(5)\n"
    "v_mfma_f32_16x16x32_bf16 v[24:27], v[56:59], v[92:95], v[24:27]\n"
    "ds_read_b128 v[48:51], v60 offset:1024\n"
    "s_waitcnt vmcnt(13) lgkmcnt(5)\n"
    "v_mfma_f32_16x16x32_bf16 v[12:15], v[28:31], v[96:99], v[12:15]\n"
    "ds_read_b128 v[52:55], v61\n"
    "s_waitcnt lgkmcnt(5)\n"
    "v_mfma_f32_16x16x32_bf16 v[16:19], v[32:35], v[96:99], v[16:19]\n"
    "ds_read_b128 v[56:59], v61 offset:1024\n"
    "s_waitcnt lgkmcnt(5)\n"
    "v_mfma_f32_16x16x32_bf16 v[20:23], v[36:39], v[96:99], v[20:23]\n"
    "ds_read_b128 v[28:31], v60 offset:4096\n"
    "s_waitcnt lgkmcnt(5)\n"
    "v_mfma_f32_16x16x32_bf16 v[24:27], v[40:43], v[96:99], v[24:27]\n"
    "ds_read_b128 v[32:35], v60 offset:5120\n"
    "s_waitcnt vmcnt(12) lgkmcnt(5)\n"
    "v_mfma_f32_16x16x32_bf16 v[12:15], v[44:47], v[100:103], v[12:15]\n"
    "ds_read_b128 v[36:39], v61 offset:4096\n"
    "s_waitcnt lgkmcnt(5)\n"
    "v_mfma_f32_16x16x32_bf16 v[16:19], v[48:51], v[100:103], v[16:19]\n"
    "ds_read_b128 v[40:43], v61 offset:5120\n"
    "s_waitcnt lgkmcnt(5)\n"
    "v_mfma_f32_16x16x32_bf16 v[20:23], v[52:55], v[100:103], v[20:23]\n"
    "ds_read_b128 v[44:47], v60 offset:8192\n"
    "s_waitcnt lgkmcnt(5)\n"
    "v_mfma_f32_16x16x32_bf16 v[24:27], v[56:59], v[100:103], v[24:27]\n"
    "ds_read_b128 v[48:51], v60 offset:9216\n"
    "s_waitcnt vmcnt(11) lgkmcnt(5)\n"
    "v_mfma_f32_16x16x32_bf16 v[12:15], v[28:31], v[104:107], v[12:15]\n"
    "ds_read_b128 v[52:55], v61 offset:8192\n"
    "s_waitcnt lgkmcnt(5)\n"
    "v_mfma_f32_16x16x32_bf16 v[16:19], v[32:35], v[104:107], v[16:19]\n"
    "ds_read_b128 v[56:59], v61 offset:9216\n"
    "s_waitcnt lgkmcnt(5)\n"
    "v_mfma_f32_16x16x32_bf16 v[20:23], v[36:39], v[104:107], v[20:23]\n"
    "ds_read_b128 v[28:31], v60 offset:12288\n"
    "s_waitcnt lgkmcnt(5)\n"
    "v_mfma_f32_16x16x32_bf16 v[24:27], v[40:43], v[104:107], v[24:27]\n"
    "ds_read_b128 v[32:35], v60 offset:13312\n"
    "s_waitcnt vmcnt(10) lgkmcnt(5)\n"
    "v_mfma_f32_16x16x32_bf16 v[12:15], v[44:47], v[108:111], v[12:15]\n"
    "ds_read_b128 v[36:39], v61 offset:12288\n"
    "s_waitcnt lgkmcnt(5)\n"
    "v_mfma_f32_16x16x32_bf16 v[16:19], v[48:51], v[108:111], v[16:19]\n"
    "ds_read_b128 v[40:43], v61 offset:13312\n"
    "s_waitcnt lgkmcnt(5)\n"
    "v_mfma_f32_16x16x32_bf16 v[20:23], v[52:55], v[108:111], v[20:23]\n"
    "ds_read_b128 v[44:47], v60 offset:16384\n"
    "s_waitcnt lgkmcnt(5)\n"
    "v_mfma_f32_16x16x32_bf16 v[24:27], v[56:59], v[108:111], v[24:27]\n"
    "ds_read_b128 v[48:51], v60 offset:17408\n"
    "s_waitcnt vmcnt(9) lgkmcnt(5)\n"
    "v_mfma_f32_16x16x32_bf16 v[12:15], v[28:31], v[112:115], v[12:15]\n"
    "ds_read_b128 v[52:55], v61 offset:16384\n"
    "s_waitcnt lgkmcnt(5)\n"
    "v_mfma_f32_16x16x32_bf16 v[16:19], v[32:35], v[112:115], v[16:19]\n"
    "ds_read_b128 v[56:59], v61 offset:17408\n"
    "s_waitcnt lgkmcnt(5)\n"
    "v_mfma_f32_16x16x32_bf16 v[20:23], v[36:39], v[112:115], v[20:23]\n"
    "ds_read_b128 v[28:31], v60 offset:20480\n"
    "s_waitcnt lgkmcnt(5)\n"
    "v_mfma_f32_16x16x32_bf16 v[24:27], v[40:43], v[112:115], v[24:27]\n"
    "ds_read_b128 v[32:35], v60 offset:21504\n"
    "s_waitcnt vmcnt(8) lgkmcnt(5)\n"
    "v_mfma_f32_16x16x32_bf16 v[12:15], v[44:47], v[116:119], v[12:15]\n"
    "ds_read_b128 v[36:39], v61 offset:20480\n"
    "s_waitcnt lgkmcnt(5)\n"
    "v_mfma_f32_16x16x32_bf16 v[16:19], v[48:51], v[116:119], v[16:19]\n"
    "ds_read_b128 v[40:43], v61 offset:21504\n"
    "s_waitcnt lgkmcnt(5)\n"
    "v_mfma_f32_16x16x32_bf16 v[20:23], v[52:55], v[116:119], v[20:23]\n"
    "ds_read_b128 v[44:47], v60 offset:24576\n"
    "s_waitcnt lgkmcnt(5)\n"
    "v_mfma_f32_16x16x32_bf16 v[24:27], v[56:59], v[116:119], v[24:27]\n"
    "ds_read_b128 v[48:51], v60 offset:25600\n"
    "s_waitcnt vmcnt(7) lgkmcnt(5)\n"
    "v_mfma_f32_16x16x32_bf16 v[12:15], v[28:31], v[120:123], v[12:15]\n"
    "ds_read_b128 v[52:55], v61 offset:24576\n"
    "s_waitcnt lgkmcnt(5)\n"
    "v_mfma_f32_16x16x32_bf16 v[16:19], v[32:35], v[120:123], v[16:19]\n"
    "ds_read_b128 v[56:59], v61 offset:25600\n"
    "s_waitcnt lgkmcnt(5)\n"
    "v_mfma_f32_16x16x32_bf16 v[20:23], v[36:39], v[120:123], v[20:23]\n"
    "ds_read_b128 v[28:31], v60 offset:28672\n"
    "s_waitcnt lgkmcnt(5)\n"
    "v_mfma_f32_16x16x32_bf16 v[24:27], v[40:43], v[120:123], v[24:27]\n"
    "ds_read_b128 v[32:35], v60 offset:29696\n"
    "s_waitcnt vmcnt(6) lgkmcnt(5)\n"
    "v_mfma_f32_16x16x32_bf16 v[12:15], v[44:47], v[124:127], v[12:15]\n"
    "ds_read_b128 v[36:39], v61 offset:28672\n"
    "s_waitcnt lgkmcnt(5)\n"
    "v_mfma_f32_16x16x32_bf16 v[16:19], v[48:51], v[124:127], v[16:19]\n"
    "ds_read_b128 v[40:43], v61 offset:29696\n"
    "s_waitcnt lgkmcnt(5)\n"
    "v_mfma_f32_16x16x32_bf16 v[20:23], v[52:55], v[124:127], v[20:23]\n"
    "ds_read_b128 v[44:47], v60 offset:32768\n"
    "s_waitcnt lgkmcnt(5)\n"
    "v_mfma_f32_16x16x32_bf16 v[24:27], v[56:59], v[124:127], v[24:27]\n"
    "ds_read_b128 v[48:51], v60 offset:33792\n"
    "s_waitcnt vmcnt(5) lgkmcnt(5)\n"
    "v_mfma_f32_16x16x32_bf16 v[12:15], v[28:31], v[128:131], v[12:15]\n"
    "ds_read_b128 v[52:55], v61 offset:32768\n"
    "s_waitcnt lgkmcnt(5)\n"
    "v_mfma_f32_16x16x32_bf16 v[16:19], v[32:35], v[128:131], v[16:19]\n"
    "ds_read_b128 v[56:59], v61 offset:33792\n"
    "s_waitcnt lgkmcnt(5)\n"
    "v_mfma_f32_16x16x32_bf16 v[20:23], v[36:39], v[128:131], v[20:23]\n"
    "ds_read_b128 v[28:31], v60 offset:36864\n"
    "s_waitcnt lgkmcnt(5)\n"
    "v_mfma_f32_16x16x32_bf16 v[24:27], v[40:43], v[128:131], v[24:27]\n"
    "ds_read_b128 v[32:35], v60 offset:37888\n"
    "s_waitcnt vmcnt(4) lgkmcnt(5)\n"
    "v_mfma_f32_16x16x32_bf16 v[12:15], v[44:47], v[132:135], v[12:15]\n"
    "ds_read_b128 v[36:39], v61 offset:36864\n"
    "s_waitcnt lgkmcnt(5)\n"
    "v_mfma_f32_16x16x32_bf16 v[16:19], v[48:51], v[132:135], v[16:19]\n"
    "ds_read_b128 v[40:43], v61 offset:37888\n"
    "s_waitcnt lgkmcnt(5)\n"
    "v_mfma_f32_16x16x32_bf16 v[20:23], v[52:55], v[132:135], v[20:23]\n"
    "s_barrier\n"
    "ds_read_b128 v[44:47], v8\n"
    "s_waitcnt lgkmcnt(5)\n"
    "v_mfma_f32_16x16x32_bf16 v[24:27], v[56:59], v[132:135], v[24:27]\n"
    "ds_read_b128 v[48:51], v8 offset:1024\n"
    "s_waitcnt vmcnt(3) lgkmcnt(5)\n"
    "v_mfma_f32_16x16x32_bf16 v[12:15], v[28:31], v[136:139], v[12:15]\n"
    "ds_read_b128 v[52:55], v9\n"
    "s_waitcnt lgkmcnt(5)\n"
    "v_mfma_f32_16x16x32_bf16 v[16:19], v[32:35], v[136:139], v[16:19]\n"
    "ds_read_b128 v[56:59], v9 offset:1024\n"
    "s_waitcnt lgkmcnt(5)\n"
    "v_mfma_f32_16x16x32_bf16 v[20:23], v[36:39], v[136:139], v[20:23]\n"
    "ds_read_b128 v[28:31], v8 offset:4096\n"
    "s_waitcnt lgkmcnt(5)\n"
    "v_mfma_f32_16x16x32_bf16 v[24:27], v[40:43], v[136:139], v[24:27]\n"
    "ds_read_b128 v[32:35], v8 offset:5120\n"
    "s_waitcnt vmcnt(2) lgkmcnt(5)\n"
    "v_mfma_f32_16x16x32_bf16 v[12:15], v[44:47], v[140:143], v[12:15]\n"
    "ds_read_b128 v[36:39], v9 offset:4096\n"
    "s_waitcnt lgkmcnt(5)\n"
    "v_mfma_f32_16x16x32_bf16 v[16:19], v[48:51], v[140:143], v[16:19]\n"
    "ds_read_b128 v[40:43], v9 offset:5120\n"
    "s_waitcnt lgkmcnt(5)\n"
    "v_mfma_f32_16x16x32_bf16 v[20:23], v[52:55], v[140:143], v[20:23]\n"
    "ds_read_b128 v[44:47], v8 offset:8192\n"
    "s_waitcnt lgkmcnt(5)\n"
    "v_mfma_f32_16x16x32_bf16 v[24:27], v[56:59], v[140:143], v[24:27]\n"
    "ds_read_b128 v[48:51], v8 offset:9216\n"
    "s_waitcnt vmcnt(1) lgkmcnt(5)\n"
    "v_mfma_f32_16x16x32_bf16 v[12:15], v[28:31], v[144:147], v[12:15]\n"
    "ds_read_b128 v[52:55], v9 offset:8192\n"
    "s_waitcnt lgkmcnt(5)\n"
    "v_mfma_f32_16x16x32_bf16 v[16:19], v[32:35], v[144:147], v[16:19]\n"
    "ds_read_b128 v[56:59], v9 offset:9216\n"
    "s_waitcnt lgkmcnt(5)\n"
    "v_mfma_f32_16x16x32_bf16 v[20:23], v[36:39], v[144:147], v[20:23]\n"
    "s_waitcnt lgkmcnt(4)\n"
    "v_mfma_f32_16x16x32_bf16 v[24:27], v[40:43], v[144:147], v[24:27]\n"
    "s_waitcnt vmcnt(0) lgkmcnt(3)\n"
    "v_mfma_f32_16x16x32_bf16 v[12:15], v[44:47], v[148:151], v[12:15]\n"
    "s_waitcnt lgkmcnt(2)\n"
    "v_mfma_f32_16x16x32_bf16 v[16:19], v[48:51], v[148:151], v[16:19]\n"
    "s_waitcnt lgkmcnt(1)\n"
    "v_mfma_f32_16x16x32_bf16 v[20:23], v[52:55], v[148:151], v[20:23]\n"
    "s_waitcnt lgkmcnt(0)\n"
    "v_mfma_f32_16x16x32_bf16 v[24:27], v[56:59], v[148:151], v[24:27]\n"
    "s_nop 5\n"
    "ds_write_b128 v10, v[20:23]\n"
    "s_nop 0\n"
    "ds_write_b128 v10, v[24:27] offset:1024\n"
    "v_cmp_u_f32 vcc, v24, v24\n"
    "s_cmp_eq_u32 s76, 0\n"
    "s_cselect_b64 vcc, vcc, 0\n"
    "s_cbranch_vccnz .Lp4_slow\n"
    ".Lp4_cont:\n"
    "s_waitcnt lgkmcnt(0)\n"
    "s_barrier\n"
    "ds_read_b128 v[240:243], v11\n"
    "ds_read_b128 v[244:247], v11 offset:1024\n"
    "s_waitcnt lgkmcnt(1)\n"
    "v_add_f32 v12, v12, v240\n"
    "v_add_f32 v13, v13, v241\n"
    "v_add_f32 v14, v14, v242\n"
    "v_add_f32 v15, v15, v243\n"
    "v_cvt_pk_bf16_f32 v4, v12, v13\n"
    "v_cvt_pk_bf16_f32 v5, v14, v15\n"
    "s_waitcnt lgkmcnt(0)\n"
    "v_add_f32 v16, v16, v244\n"
    "v_add_f32 v17, v17, v245\n"
    "v_add_f32 v18, v18, v246\n"
    "v_add_f32 v19, v19, v247\n"
    "v_cvt_pk_bf16_f32 v6, v16, v17\n"
    "v_cvt_pk_bf16_f32 v7, v18, v19\n"
    "s_lshl_b32 s60, s50, 2\n"
    "s_add_u32 s60, s60, s53\n"
    "s_lshl_b32 s60, s60, 14\n"
    "s_lshl_b32 s61, s51, 7\n"
    "s_add_u32 s60, s60, s61\n"
    "s_lshl_b32 s61, s57, 6\n"
    "s_add_u32 s60, s60, s61\n"
    "buffer_store_dwordx4 v[4:7], v62, s[40:43], s60 offen\n"
    "s_nop 1\n"
    "s_branch .Lend\n"
    ".Lpoll_slow:\n"
    "s_mov_b32 s79, 0\n"
    ".Lpoll_loop:\n"
    "s_mov_b32 s60, 0xe88400\n"
    "buffer_load_dwordx4 v[156:159], v1, s[36:39], s60 offen sc1\n"
    "s_waitcnt vmcnt(0)\n"
    "v_xor_b32 v156, s56, v156\n"
    "v_xor_b32 v157, s56, v157\n"
    "v_xor_b32 v158, s56, v158\n"
    "v_xor_b32 v159, s56, v159\n"
    "v_or3_b32 v156, v156, v157, v158\n"
    "v_or_b32 v156, v156, v159\n"
    "v_cmp_ne_u32 vcc, 0, v156\n"
    "s_nop 4\n"
    "s_cbranch_vccz .Lpoll_ok\n"
    "s_add_u32 s79, s79, 1\n"
    "s_cmp_lt_u32 s79, 0x40000\n"
    "s_cbranch_scc1 .Lpoll_loop\n"
    "s_branch .Lpoll_ok\n"
    ".Lp2_slow:\n"
    "s_nop 7\n"
    "s_mov_b32 s79, 0\n"
    "s_mov_b32 s76, 1\n"
    ".Lp2_slow_l:\n"
    "buffer_load_dwordx4 v[40:43], v1, s[36:39], s69 offen sc1\n"
    "buffer_load_dwordx4 v[44:47], v1, s[36:39], s69 offen offset:1024 sc1\n"
    "buffer_load_dwordx4 v[48:51], v1, s[36:39], s69 offen offset:2048 sc1\n"
    "buffer_load_dwordx4 v[52:55], v1, s[36:39], s69 offen offset:3072 sc1\n"
    "buffer_load_dwordx4 v[56:59], v1, s[36:39], s70 offen sc1\n"
    "buffer_load_dwordx4 v[60:63], v1, s[36:39], s70 offen offset:1024 sc1\n"
    "buffer_load_dwordx4 v[64:67], v1, s[36:39], s70 offen offset:2048 sc1\n"
    "buffer_load_dwordx4 v[68:71], v1, s[36:39], s70 offen offset:3072 sc1\n"
    "s_waitcnt vmcnt(0)\n"
    "v_max3_u32 v120, v40, v41, v42\n"
    "v_max3_u32 v120, v120, v43, v44\n"
    "v_max3_u32 v120, v120, v45, v46\n"
    "v_max3_u32 v120, v120, v47, v48\n"
    "v_max3_u32 v120, v120, v49, v50\n"
    "v_max3_u32 v120, v120, v51, v52\n"
    "v_max3_u32 v120, v120, v53, v54\n"
    "v_max3_u32 v120, v120, v55, v56\n"
    "v_max3_u32 v120, v120, v57, v58\n"
    "v_max3_u32 v120, v120, v59, v60\n"
    "v_max3_u32 v120, v120, v61, v62\n"
    "v_max3_u32 v120, v120, v63, v64\n"
    "v_max3_u32 v120, v120, v65, v66\n"
    "v_max3_u32 v120, v120, v67, v68\n"
    "v_max3_u32 v120, v120, v69, v70\n"
    "v_max_u32 v120, v120, v71\n"
    "v_cmp_eq_u32 vcc, -1, v120\n"
    "s_waitcnt lgkmcnt(0)\n"
    "s_nop 3\n"
    "s_cbranch_vccz .Lp2_re\n"
    "s_add_u32 s79, s79, 1\n"
    "s_cmp_lt_u32 s79, 0x40000\n"
    "s_cbranch_scc1 .Lp2_slow_l\n"
    "s_branch .Lp2_re\n"
    ".Lp2_re:\n"
    "ds_read_b128 v[88:91], v8\n"
    "ds_read_b128 v[92:95], v8 offset:1024\n"
    "ds_read_b128 v[96:99], v9\n"
    "ds_read_b128 v[100:103], v9 offset:1024\n"
    "ds_read_b128 v[104:107], v8 offset:4096\n"
    "ds_read_b128 v[108:111], v8 offset:5120\n"
    "v_lshlrev_b32 v72, 16, v4\n"
    "v_and_b32 v73, 0xffff0000, v4\n"
    "v_lshlrev_b32 v74, 16, v5\n"
    "v_and_b32 v75, 0xffff0000, v5\n"
    "v_lshlrev_b32 v76, 16, v6\n"
    "v_and_b32 v77, 0xffff0000, v6\n"
    "v_lshlrev_b32 v78, 16, v7\n"
    "v_and_b32 v79, 0xffff0000, v7\n"
    "s_waitcnt lgkmcnt(5)\n"
    "v_mfma_f32_16x16x32_bf16 v[72:75], v[88:91], v[40:43], v[72:75]\n"
    "ds_read_b128 v[112:115], v9 offset:4096\n"
    "s_waitcnt lgkmcnt(5)\n"
    "v_mfma_f32_16x16x32_bf16 v[76:79], v[92:95], v[40:43], v[76:79]\n"
    "ds_read_b128 v[116:119], v9 offset:5120\n"
    "s_waitcnt lgkmcnt(5)\n"
    "v_mfma_f32_16x16x32_bf16 v[80:83], v[96:99], v[40:43], 0\n"
    "ds_read_b128 v[88:91], v8 offset:8192\n"
    "s_waitcnt lgkmcnt(5)\n"
    "v_mfma_f32_16x16x32_bf16 v[84:87], v[100:103], v[40:43], 0\n"
    "ds_read_b128 v[92:95], v8 offset:9216\n"
    "s_waitcnt lgkmcnt(5)\n"
    "v_mfma_f32_16x16x32_bf16 v[72:75], v[104:107], v[44:47], v[72:75]\n"
    "ds_read_b128 v[96:99], v9 offset:8192\n"
    "s_waitcnt lgkmcnt(5)\n"
    "v_mfma_f32_16x16x32_bf16 v[76:79], v[108:111], v[44:47], v[76:79]\n"
    "ds_read_b128 v[100:103], v9 offset:9216\n"
    "s_waitcnt lgkmcnt(5)\n"
    "v_mfma_f32_16x16x32_bf16 v[80:83], v[112:115], v[44:47], v[80:83]\n"
    "ds_read_b128 v[104:107], v8 offset:12288\n"
    "s_waitcnt lgkmcnt(5)\n"
    "v_mfma_f32_16x16x32_bf16 v[84:87], v[116:119], v[44:47], v[84:87]\n"
    "ds_read_b128 v[108:111], v8 offset:13312\n"
    "s_waitcnt lgkmcnt(5)\n"
    "v_mfma_f32_16x16x32_bf16 v[72:75], v[88:91], v[48:51], v[72:75]\n"
    "ds_read_b128 v[112:115], v9 offset:12288\n"
    "s_waitcnt lgkmcnt(5)\n"
    "v_mfma_f32_16x16x32_bf16 v[76:79], v[92:95], v[48:51], v[76:79]\n"
    "ds_read_b128 v[116:119], v9 offset:13312\n"
    "s_waitcnt lgkmcnt(5)\n"
    "v_mfma_f32_16x16x32_bf16 v[80:83], v[96:99], v[48:51], v[80:83]\n"
    "ds_read_b128 v[88:91], v8 offset:16384\n"
    "s_waitcnt lgkmcnt(5)\n"
    "v_mfma_f32_16x16x32_bf16 v[84:87], v[100:103], v[48:51], v[84:87]\n"
    "ds_read_b128 v[92:95], v8 offset:17408\n"
    "s_waitcnt lgkmcnt(5)\n"
    "v_mfma_f32_16x16x32_bf16 v[72:75], v[104:107], v[52:55], v[72:75]\n"
    "ds_read_b128 v[96:99], v9 offset:16384\n"
    "s_waitcnt lgkmcnt(5)\n"
    "v_mfma_f32_16x16x32_bf16 v[76:79], v[108:111], v[52:55], v[76:79]\n"
    "ds_read_b128 v[100:103], v9 offset:17408\n"
    "s_waitcnt lgkmcnt(5)\n"
    "v_mfma_f32_16x16x32_bf16 v[80:83], v[112:115], v[52:55], v[80:83]\n"
    "ds_read_b128 v[104:107], v8 offset:20480\n"
    "s_waitcnt lgkmcnt(5)\n"
    "v_mfma_f32_16x16x32_bf16 v[84:87], v[116:119], v[52:55], v[84:87]\n"
    "ds_read_b128 v[108:111], v8 offset:21504\n"
    "s_waitcnt lgkmcnt(5)\n"
    "v_mfma_f32_16x16x32_bf16 v[72:75], v[88:91], v[56:59], v[72:75]\n"
    "ds_read_b128 v[112:115], v9 offset:20480\n"
    "s_waitcnt lgkmcnt(5)\n"
    "v_mfma_f32_16x16x32_bf16 v[76:79], v[92:95], v[56:59], v[76:79]\n"
    "ds_read_b128 v[116:119], v9 offset:21504\n"
    "s_waitcnt lgkmcnt(5)\n"
    "v_mfma_f32_16x16x32_bf16 v[80:83], v[96:99], v[56:59], v[80:83]\n"
    "ds_read_b128 v[88:91], v8 offset:24576\n"
    "s_waitcnt lgkmcnt(5)\n"
    "v_mfma_f32_16x16x32_bf16 v[84:87], v[100:103], v[56:59], v[84:87]\n"
    "ds_read_b128 v[92:95], v8 offset:25600\n"
    "s_waitcnt lgkmcnt(5)\n"
    "v_mfma_f32_16x16x32_bf16 v[72:75], v[104:107], v[60:63], v[72:75]\n"
    "ds_read_b128 v[96:99], v9 offset:24576\n"
    "s_waitcnt lgkmcnt(5)\n"
    "v_mfma_f32_16x16x32_bf16 v[76:79], v[108:111], v[60:63], v[76:79]\n"
    "ds_read_b128 v[100:103], v9 offset:25600\n"
    "s_waitcnt lgkmcnt(5)\n"
    "v_mfma_f32_16x16x32_bf16 v[80:83], v[112:115], v[60:63], v[80:83]\n"
    "ds_read_b128 v[104:107], v8 offset:28672\n"
    "s_waitcnt lgkmcnt(5)\n"
    "v_mfma_f32_16x16x32_bf16 v[84:87], v[116:119], v[60:63], v[84:87]\n"
    "ds_read_b128 v[108:111], v8 offset:29696\n"
    "s_waitcnt lgkmcnt(5)\n"
    "v_mfma_f32_16x16x32_bf16 v[72:75], v[88:91], v[64:67], v[72:75]\n"
    "ds_read_b128 v[112:115], v9 offset:28672\n"
    "s_waitcnt lgkmcnt(5)\n"
    "v_mfma_f32_16x16x32_bf16 v[76:79], v[92:95], v[64:67], v[76:79]\n"
    "ds_read_b128 v[116:119], v9 offset:29696\n"
    "s_waitcnt lgkmcnt(5)\n"
    "v_mfma_f32_16x16x32_bf16 v[80:83], v[96:99], v[64:67], v[80:83]\n"
    "s_waitcnt lgkmcnt(4)\n"
    "v_mfma_f32_16x16x32_bf16 v[84:87], v[100:103], v[64:67], v[84:87]\n"
    "s_waitcnt lgkmcnt(3)\n"
    "v_mfma_f32_16x16x32_bf16 v[72:75], v[104:107], v[68:71], v[72:75]\n"
    "s_waitcnt lgkmcnt(2)\n"
    "v_mfma_f32_16x16x32_bf16 v[76:79], v[108:111], v[68:71], v[76:79]\n"
    "s_waitcnt lgkmcnt(1)\n"
    "v_mfma_f32_16x16x32_bf16 v[80:83], v[112:115], v[68:71], v[80:83]\n"
    "s_waitcnt lgkmcnt(0)\n"
    "v_mfma_f32_16x16x32_bf16 v[84:87], v[116:119], v[68:71], v[84:87]\n"
    "s_nop 5\n"
    "ds_write_b128 v10, v[80:83]\n"
    "s_nop 0\n"
    "ds_write_b128 v10, v[84:87] offset:1024\n"
    "s_nop 7\n"
    "s_branch .Lp2_cont\n"
    ".Lh_slow0:\n"
    "s_nop 7\n"
    "s_waitcnt vmcnt(0)\n"
    "s_mov_b32 s79, 0\n"
    "s_add_u32 s60, s69, 0\n"
    ".Lh_slow0_l:\n"
    "buffer_load_dwordx4 v[64:67], v1, s[36:39], s60 offen sc1\n"
    "buffer_load_dwordx4 v[68:71], v1, s[36:39], s60 offen offset:1024 sc1\n"
    "s_waitcnt vmcnt(0)\n"
    "v_max3_u32 v11, v64, v65, v66\n"
    "v_max3_u32 v11, v11, v67, v68\n"
    "v_max3_u32 v11, v11, v69, v70\n"
    "v_max_u32 v11, v11, v71\n"
    "v_cmp_eq_u32 vcc, -1, v11\n"
    "s_nop 4\n"
    "s_cbranch_vccz .Lh_ok0\n"
    "s_add_u32 s79, s79, 1\n"
    "s_cmp_lt_u32 s79, 0x40000\n"
    "s_cbranch_scc1 .Lh_slow0_l\n"
    "s_branch .Lh_ok0\n"
    ".Lh_slow1:\n"
    "s_nop 7\n"
    "s_waitcnt vmcnt(0)\n"
    "s_mov_b32 s79, 0\n"
    "s_add_u32 s60, s69, 0x4000\n"
    ".Lh_slow1_l:\n"
    "buffer_load_dwordx4 v[72:75], v1, s[36:39], s60 offen sc1\n"
    "buffer_load_dwordx4 v[76:79], v1, s[36:39], s60 offen offset:1024 sc1\n"
    "s_waitcnt vmcnt(0)\n"
    "v_max3_u32 v11, v72, v73, v74\n"
    "v_max3_u32 v11, v11, v75, v76\n"
    "v_max3_u32 v11, v11, v77, v78\n"
    "v_max_u32 v11, v11, v79\n"
    "v_cmp_eq_u32 vcc, -1, v11\n"
    "s_nop 4\n"
    "s_cbranch_vccz .Lh_ok1\n"
    "s_add_u32 s79, s79, 1\n"
    "s_cmp_lt_u32 s79, 0x40000\n"
    "s_cbranch_scc1 .Lh_slow1_l\n"
    "s_branch .Lh_ok1\n"
    ".Lh_slow2:\n"
    "s_nop 7\n"
    "s_waitcnt vmcnt(0)\n"
    "s_mov_b32 s79, 0\n"
    "s_add_u32 s60, s69, 0x2000\n"
    ".Lh_slow2_l:\n"
    "buffer_load_dwordx4 v[80:83], v1, s[36:39], s60 offen sc1\n"
    "buffer_load_dwordx4 v[84:87], v1, s[36:39], s60 offen offset:1024 sc1\n"
    "s_waitcnt vmcnt(0)\n"
    "v_max3_u32 v11, v80, v81, v82\n"
    "v_max3_u32 v11, v11, v83, v84\n"
    "v_max3_u32 v11, v11, v85, v86\n"
    "v_max_u32 v11, v11, v87\n"
    "v_cmp_eq_u32 vcc, -1, v11\n"
    "s_nop 4\n"
    "s_cbranch_vccz .Lh_ok2\n"
    "s_add_u32 s79, s79, 1\n"
    "s_cmp_lt_u32 s79, 0x40000\n"
    "s_cbranch_scc1 .Lh_slow2_l\n"
    "s_branch .Lh_ok2\n"
    ".Lh_slow3:\n"
    "s_nop 7\n"
    "s_waitcnt vmcnt(0)\n"
    "s_mov_b32 s79, 0\n"
    "s_add_u32 s60, s69, 0x8000\n"
    ".Lh_slow3_l:\n"
    "buffer_load_dwordx4 v[88:91], v1, s[36:39], s60 offen sc1\n"
    "buffer_load_dwordx4 v[92:95], v1, s[36:39], s60 offen offset:1024 sc1\n"
    "s_waitcnt vmcnt(0)\n"
    "v_max3_u32 v11, v88, v89, v90\n"
    "v_max3_u32 v11, v11, v91, v92\n"
    "v_max3_u32 v11, v11, v93, v94\n"
    "v_max_u32 v11, v11, v95\n"
    "v_cmp_eq_u32 vcc, -1, v11\n"
    "s_nop 4\n"
    "s_cbranch_vccz .Lh_ok3\n"
    "s_add_u32 s79, s79, 1\n"
    "s_cmp_lt_u32 s79, 0x40000\n"
    "s_cbranch_scc1 .Lh_slow3_l\n"
    "s_branch .Lh_ok3\n"
    ".Lh_slow4:\n"
    "s_nop 7\n"
    "s_waitcnt vmcnt(0)\n"
    "s_mov_b32 s79, 0\n"
    "s_add_u32 s60, s69, 0x6000\n"
    ".Lh_slow4_l:\n"
    "buffer_load_dwordx4 v[96:99], v1, s[36:39], s60 offen sc1\n"
    "buffer_load_dwordx4 v[100:103], v1, s[36:39], s60 offen offset:1024 sc1\n"
    "s_waitcnt vmcnt(0)\n"
    "v_max3_u32 v11, v96, v97, v98\n"
    "v_max3_u32 v11, v11, v99, v100\n"
    "v_max3_u32 v11, v11, v101, v102\n"
    "v_max_u32 v11, v11, v103\n"
    "v_cmp_eq_u32 vcc, -1, v11\n"
    "s_nop 4\n"
    "s_cbranch_vccz .Lh_ok4\n"
    "s_add_u32 s79, s79, 1\n"
    "s_cmp_lt_u32 s79, 0x40000\n"
    "s_cbranch_scc1 .Lh_slow4_l\n"
    "s_branch .Lh_ok4\n"
    ".Lh_slow5:\n"
    "s_nop 7\n"
    "s_waitcnt vmcnt(0)\n"
    "s_mov_b32 s79, 0\n"
    "s_add_u32 s60, s69, 0xc000\n"
    ".Lh_slow5_l:\n"
    "buffer_load_dwordx4 v[104:107], v1, s[36:39], s60 offen sc1\n"
    "buffer_load_dwordx4 v[108:111], v1, s[36:39], s60 offen offset:1024 sc1\n"
    "s_waitcnt vmcnt(0)\n"
    "v_max3_u32 v11, v104, v105, v106\n"
    "v_max3_u32 v11, v11, v107, v108\n"
    "v_max3_u32 v11, v11, v109, v110\n"
    "v_max_u32 v11, v11, v111\n"
    "v_cmp_eq_u32 vcc, -1, v11\n"
    "s_nop 4\n"
    "s_cbranch_vccz .Lh_ok5\n"
    "s_add_u32 s79, s79, 1\n"
    "s_cmp_lt_u32 s79, 0x40000\n"
    "s_cbranch_scc1 .Lh_slow5_l\n"
    "s_branch .Lh_ok5\n"
    ".Lh_slow6:\n"
    "s_nop 7\n"
    "s_waitcnt vmcnt(0)\n"
    "s_mov_b32 s79, 0\n"
    "s_add_u32 s60, s69, 0xa000\n"
    ".Lh_slow6_l:\n"
    "buffer_load_dwordx4 v[64:67], v1, s[36:39], s60 offen sc1\n"
    "buffer_load_dwordx4 v[68:71], v1, s[36:39], s60 offen offset:1024 sc1\n"
    "s_waitcnt vmcnt(0)\n"
    "v_max3_u32 v11, v64, v65, v66\n"
    "v_max3_u32 v11, v11, v67, v68\n"
    "v_max3_u32 v11, v11, v69, v70\n"
    "v_max_u32 v11, v11, v71\n"
    "v_cmp_eq_u32 vcc, -1, v11\n"
    "s_nop 4\n"
    "s_cbranch_vccz .Lh_ok6\n"
    "s_add_u32 s79, s79, 1\n"
    "s_cmp_lt_u32 s79, 0x40000\n"
    "s_cbranch_scc1 .Lh_slow6_l\n"
    "s_branch .Lh_ok6\n"
    ".Lh_slow7:\n"
    "s_nop 7\n"
    "s_waitcnt vmcnt(0)\n"
    "s_mov_b32 s79, 0\n"
    "s_add_u32 s60, s69, 0x10000\n"
    ".Lh_slow7_l:\n"
    "buffer_load_dwordx4 v[72:75], v1, s[36:39], s60 offen sc1\n"
    "buffer_load_dwordx4 v[76:79], v1, s[36:39], s60 offen offset:1024 sc1\n"
    "s_waitcnt vmcnt(0)\n"
    "v_max3_u32 v11, v72, v73, v74\n"
    "v_max3_u32 v11, v11, v75, v76\n"
    "v_max3_u32 v11, v11, v77, v78\n"
    "v_max_u32 v11, v11, v79\n"
    "v_cmp_eq_u32 vcc, -1, v11\n"
    "s_nop 4\n"
    "s_cbranch_vccz .Lh_ok7\n"
    "s_add_u32 s79, s79, 1\n"
    "s_cmp_lt_u32 s79, 0x40000\n"
    "s_cbranch_scc1 .Lh_slow7_l\n"
    "s_branch .Lh_ok7\n"
    ".Lh_slow8:\n"
    "s_nop 7\n"
    "s_waitcnt vmcnt(0)\n"
    "s_mov_b32 s79, 0\n"
    "s_add_u32 s60, s69, 0xe000\n"
    ".Lh_slow8_l:\n"
    "buffer_load_dwordx4 v[80:83], v1, s[36:39], s60 offen sc1\n"
    "buffer_load_dwordx4 v[84:87], v1, s[36:39], s60 offen offset:1024 sc1\n"
    "s_waitcnt vmcnt(0)\n"
    "v_max3_u32 v11, v80, v81, v82\n"
    "v_max3_u32 v11, v11, v83, v84\n"
    "v_max3_u32 v11, v11, v85, v86\n"
    "v_max_u32 v11, v11, v87\n"
    "v_cmp_eq_u32 vcc, -1, v11\n"
    "s_nop 4\n"
    "s_cbranch_vccz .Lh_ok8\n"
    "s_add_u32 s79, s79, 1\n"
    "s_cmp_lt_u32 s79, 0x40000\n"
    "s_cbranch_scc1 .Lh_slow8_l\n"
    "s_branch .Lh_ok8\n"
    ".Lh_slow9:\n"
    "s_nop 7\n"
    "s_waitcnt vmcnt(0)\n"
    "s_mov_b32 s79, 0\n"
    "s_add_u32 s60, s69, 0x14000\n"
    ".Lh_slow9_l:\n"
    "buffer_load_dwordx4 v[88:91], v1, s[36:39], s60 offen sc1\n"
    "buffer_load_dwordx4 v[92:95], v1, s[36:39], s60 offen offset:1024 sc1\n"
    "s_waitcnt vmcnt(0)\n"
    "v_max3_u32 v11, v88, v89, v90\n"
    "v_max3_u32 v11, v11, v91, v92\n"
    "v_max3_u32 v11, v11, v93, v94\n"
    "v_max_u32 v11, v11, v95\n"
    "v_cmp_eq_u32 vcc, -1, v11\n"
    "s_nop 4\n"
    "s_cbranch_vccz .Lh_ok9\n"
    "s_add_u32 s79, s79, 1\n"
    "s_cmp_lt_u32 s79, 0x40000\n"
    "s_cbranch_scc1 .Lh_slow9_l\n"
    "s_branch .Lh_ok9\n"
    ".Lh_slow10:\n"
    "s_nop 7\n"
    "s_waitcnt vmcnt(0)\n"
    "s_mov_b32 s79, 0\n"
    "s_add_u32 s60, s69, 0x12000\n"
    ".Lh_slow10_l:\n"
    "buffer_load_dwordx4 v[96:99], v1, s[36:39], s60 offen sc1\n"
    "buffer_load_dwordx4 v[100:103], v1, s[36:39], s60 offen offset:1024 sc1\n"
    "s_waitcnt vmcnt(0)\n"
    "v_max3_u32 v11, v96, v97, v98\n"
    "v_max3_u32 v11, v11, v99, v100\n"
    "v_max3_u32 v11, v11, v101, v102\n"
    "v_max_u32 v11, v11, v103\n"
    "v_cmp_eq_u32 vcc, -1, v11\n"
    "s_nop 4\n"
    "s_cbranch_vccz .Lh_ok10\n"
    "s_add_u32 s79, s79, 1\n"
    "s_cmp_lt_u32 s79, 0x40000\n"
    "s_cbranch_scc1 .Lh_slow10_l\n"
    "s_branch .Lh_ok10\n"
    ".Lh_slow11:\n"
    "s_nop 7\n"
    "s_waitcnt vmcnt(0)\n"
    "s_mov_b32 s79, 0\n"
    "s_add_u32 s60, s69, 0x18000\n"
    ".Lh_slow11_l:\n"
    "buffer_load_dwordx4 v[104:107], v1, s[36:39], s60 offen sc1\n"
    "buffer_load_dwordx4 v[108:111], v1, s[36:39], s60 offen offset:1024 sc1\n"
    "s_waitcnt vmcnt(0)\n"
    "v_max3_u32 v11, v104, v105, v106\n"
    "v_max3_u32 v11, v11, v107, v108\n"
    "v_max3_u32 v11, v11, v109, v110\n"
    "v_max_u32 v11, v11, v111\n"
    "v_cmp_eq_u32 vcc, -1, v11\n"
    "s_nop 4\n"
    "s_cbranch_vccz .Lh_ok11\n"
    "s_add_u32 s79, s79, 1\n"
    "s_cmp_lt_u32 s79, 0x40000\n"
    "s_cbranch_scc1 .Lh_slow11_l\n"
    "s_branch .Lh_ok11\n"
    ".Lh_slow12:\n"
    "s_nop 7\n"
    "s_waitcnt vmcnt(0)\n"
    "s_mov_b32 s79, 0\n"
    "s_add_u32 s60, s69, 0x16000\n"
    ".Lh_slow12_l:\n"
    "buffer_load_dwordx4 v[64:67], v1, s[36:39], s60 offen sc1\n"
    "buffer_load_dwordx4 v[68:71], v1, s[36:39], s60 offen offset:1024 sc1\n"
    "s_waitcnt vmcnt(0)\n"
    "v_max3_u32 v11, v64, v65, v66\n"
    "v_max3_u32 v11, v11, v67, v68\n"
    "v_max3_u32 v11, v11, v69, v70\n"
    "v_max_u32 v11, v11, v71\n"
    "v_cmp_eq_u32 vcc, -1, v11\n"
    "s_nop 4\n"
    "s_cbranch_vccz .Lh_ok12\n"
    "s_add_u32 s79, s79, 1\n"
    "s_cmp_lt_u32 s79, 0x40000\n"
    "s_cbranch_scc1 .Lh_slow12_l\n"
    "s_branch .Lh_ok12\n"
    ".Lh_slow13:\n"
    "s_nop 7\n"
    "s_waitcnt vmcnt(0)\n"
    "s_mov_b32 s79, 0\n"
    "s_add_u32 s60, s69, 0x1c000\n"
    ".Lh_slow13_l:\n"
    "buffer_load_dwordx4 v[72:75], v1, s[36:39], s60 offen sc1\n"
    "buffer_load_dwordx4 v[76:79], v1, s[36:39], s60 offen offset:1024 sc1\n"
    "s_waitcnt vmcnt(0)\n"
    "v_max3_u32 v11, v72, v73, v74\n"
    "v_max3_u32 v11, v11, v75, v76\n"
    "v_max3_u32 v11, v11, v77, v78\n"
    "v_max_u32 v11, v11, v79\n"
    "v_cmp_eq_u32 vcc, -1, v11\n"
    "s_nop 4\n"
    "s_cbranch_vccz .Lh_ok13\n"
    "s_add_u32 s79, s79, 1\n"
    "s_cmp_lt_u32 s79, 0x40000\n"
    "s_cbranch_scc1 .Lh_slow13_l\n"
    "s_branch .Lh_ok13\n"
    ".Lh_slow14:\n"
    "s_nop 7\n"
    "s_waitcnt vmcnt(0)\n"
    "s_mov_b32 s79, 0\n"
    "s_add_u32 s60, s69, 0x1a000\n"
    ".Lh_slow14_l:\n"
    "buffer_load_dwordx4 v[80:83], v1, s[36:39], s60 offen sc1\n"
    "buffer_load_dwordx4 v[84:87], v1, s[36:39], s60 offen offset:1024 sc1\n"
    "s_waitcnt vmcnt(0)\n"
    "v_max3_u32 v11, v80, v81, v82\n"
    "v_max3_u32 v11, v11, v83, v84\n"
    "v_max3_u32 v11, v11, v85, v86\n"
    "v_max_u32 v11, v11, v87\n"
    "v_cmp_eq_u32 vcc, -1, v11\n"
    "s_nop 4\n"
    "s_cbranch_vccz .Lh_ok14\n"
    "s_add_u32 s79, s79, 1\n"
    "s_cmp_lt_u32 s79, 0x40000\n"
    "s_cbranch_scc1 .Lh_slow14_l\n"
    "s_branch .Lh_ok14\n"
    ".Lh_slow15:\n"
    "s_nop 7\n"
    "s_waitcnt vmcnt(0)\n"
    "s_mov_b32 s79, 0\n"
    "s_add_u32 s60, s69, 0x1e000\n"
    ".Lh_slow15_l:\n"
    "buffer_load_dwordx4 v[88:91], v1, s[36:39], s60 offen sc1\n"
    "buffer_load_dwordx4 v[92:95], v1, s[36:39], s60 offen offset:1024 sc1\n"
    "s_waitcnt vmcnt(0)\n"
    "v_max3_u32 v11, v88, v89, v90\n"
    "v_max3_u32 v11, v11, v91, v92\n"
    "v_max3_u32 v11, v11, v93, v94\n"
    "v_max_u32 v11, v11, v95\n"
    "v_cmp_eq_u32 vcc, -1, v11\n"
    "s_nop 4\n"
    "s_cbranch_vccz .Lh_ok15\n"
    "s_add_u32 s79, s79, 1\n"
    "s_cmp_lt_u32 s79, 0x40000\n"
    "s_cbranch_scc1 .Lh_slow15_l\n"
    "s_branch .Lh_ok15\n"
    ".Lsv_slow:\n"
    "s_nop 7\n"
    "s_waitcnt vmcnt(0)\n"
    "s_mov_b32 s79, 0\n"
    "s_mov_b32 s60, s65\n"
    ".Lsv_slow_l:\n"
    "buffer_load_dwordx4 v[92:95], v1, s[36:39], s60 offen sc1\n"
    "buffer_load_dwordx4 v[96:99], v1, s[36:39], s60 offen offset:1024 sc1\n"
    "buffer_load_dwordx4 v[100:103], v1, s[36:39], s60 offen offset:2048 sc1\n"
    "buffer_load_dwordx4 v[104:107], v1, s[36:39], s60 offen offset:3072 sc1\n"
    "s_waitcnt vmcnt(0)\n"
    "v_max3_u32 v76, v92, v93, v94\n"
    "v_max3_u32 v76, v76, v95, v96\n"
    "v_max3_u32 v76, v76, v97, v98\n"
    "v_max3_u32 v76, v76, v99, v100\n"
    "v_max3_u32 v76, v76, v101, v102\n"
    "v_max3_u32 v76, v76, v103, v104\n"
    "v_max3_u32 v76, v76, v105, v106\n"
    "v_max_u32 v76, v76, v107\n"
    "v_cmp_eq_u32 vcc, -1, v76\n"
    "s_nop 4\n"
    "s_cbranch_vccz .Lsv_ok\n"
    "s_add_u32 s79, s79, 1\n"
    "s_cmp_lt_u32 s79, 0x40000\n"
    "s_cbranch_scc1 .Lsv_slow_l\n"
    "s_branch .Lsv_ok\n"
    ".Lp4_slow:\n"
    "s_nop 7\n"
    "s_mov_b32 s79, 0\n"
    "s_mov_b32 s76, 1\n"
    ".Lp4_slow_l:\n"
    "s_add_u32 s65, s69, 0\n"
    "buffer_load_dwordx4 v[64:67], v1, s[36:39], s65 offen sc1\n"
    "buffer_load_dwordx4 v[68:71], v1, s[36:39], s65 offen offset:1024 sc1\n"
    "buffer_load_dwordx4 v[72:75], v1, s[36:39], s65 offen offset:2048 sc1\n"
    "buffer_load_dwordx4 v[76:79], v1, s[36:39], s65 offen offset:3072 sc1\n"
    "s_add_u32 s65, s69, 0x1000\n"
    "buffer_load_dwordx4 v[80:83], v1, s[36:39], s65 offen sc1\n"
    "buffer_load_dwordx4 v[84:87], v1, s[36:39], s65 offen offset:1024 sc1\n"
    "buffer_load_dwordx4 v[88:91], v1, s[36:39], s65 offen offset:2048 sc1\n"
    "buffer_load_dwordx4 v[92:95], v1, s[36:39], s65 offen offset:3072 sc1\n"
    "s_add_u32 s65, s69, 0x2000\n"
    "buffer_load_dwordx4 v[96:99], v1, s[36:39], s65 offen sc1\n"
    "buffer_load_dwordx4 v[100:103], v1, s[36:39], s65 offen offset:1024 sc1\n"
    "buffer_load_dwordx4 v[104:107], v1, s[36:39], s65 offen offset:2048 sc1\n"
    "buffer_load_dwordx4 v[108:111], v1, s[36:39], s65 offen offset:3072 sc1\n"
    "s_add_u32 s65, s69, 0x3000\n"
    "buffer_load_dwordx4 v[112:115], v1, s[36:39], s65 offen sc1\n"
    "buffer_load_dwordx4 v[116:119], v1, s[36:39], s65 offen offset:1024 sc1\n"
    "buffer_load_dwordx4 v[120:123], v1, s[36:39], s65 offen offset:2048 sc1\n"
    "buffer_load_dwordx4 v[124:127], v1, s[36:39], s65 offen offset:3072 sc1\n"
    "s_add_u32 s65, s69, 0x4000\n"
    "buffer_load_dwordx4 v[128:131], v1, s[36:39], s65 offen sc1\n"
    "buffer_load_dwordx4 v[132:135], v1, s[36:39], s65 offen offset:1024 sc1\n"
    "buffer_load_dwordx4 v[136:139], v1, s[36:39], s65 offen offset:2048 sc1\n"
    "buffer_load_dwordx4 v[140:143], v1, s[36:39], s65 offen offset:3072 sc1\n"
    "s_add_u32 s65, s69, 0x5000\n"
    "buffer_load_dwordx4 v[144:147], v1, s[36:39], s65 offen sc1\n"
    "s_cmp_eq_u32 s57, 1\n"
    "s_cselect_b32 s65, 0x4000000, s65\n"
    "buffer_load_dwordx4 v[148:151], v1, s[36:39], s65 offen offset:1024 sc1\n"
    "s_waitcnt vmcnt(0)\n"
    "v_max3_u32 v248, v64, v65, v66\n"
    "v_max3_u32 v248, v248, v67, v68\n"
    "v_max3_u32 v248, v248, v69, v70\n"
    "v_max3_u32 v248, v248, v71, v72\n"
    "v_max3_u32 v248, v248, v73, v74\n"
    "v_max3_u32 v248, v248, v75, v76\n"
    "v_max3_u32 v248, v248, v77, v78\n"
    "v_max3_u32 v248, v248, v79, v80\n"
    "v_max3_u32 v248, v248, v81, v82\n"
    "v_max3_u32 v248, v248, v83, v84\n"
    "v_max3_u32 v248, v248, v85, v86\n"
    "v_max3_u32 v248, v248, v87, v88\n"
    "v_max3_u32 v248, v248, v89, v90\n"
    "v_max3_u32 v248, v248, v91, v92\n"
    "v_max3_u32 v248, v248, v93, v94\n"
    "v_max3_u32 v248, v248, v95, v96\n"
    "v_max3_u32 v248, v248, v97, v98\n"
    "v_max3_u32 v248, v248, v99, v100\n"
    "v_max3_u32 v248, v248, v101, v102\n"
    "v_max3_u32 v248, v248, v103, v104\n"
    "v_max3_u32 v248, v248, v105, v106\n"
    "v_max3_u32 v248, v248, v107, v108\n"
    "v_max3_u32 v248, v248, v109, v110\n"
    "v_max3_u32 v248, v248, v111, v112\n"
    "v_max3_u32 v248, v248, v113, v114\n"
    "v_max3_u32 v248, v248, v115, v116\n"
    "v_max3_u32 v248, v248, v117, v118\n"
    "v_max3_u32 v248, v248, v119, v120\n"
    "v_max3_u32 v248, v248, v121, v122\n"
    "v_max3_u32 v248, v248, v123, v124\n"
    "v_max3_u32 v248, v248, v125, v126\n"
    "v_max3_u32 v248, v248, v127, v128\n"
    "v_max3_u32 v248, v248, v129, v130\n"
    "v_max3_u32 v248, v248, v131, v132\n"
    "v_max3_u32 v248, v248, v133, v134\n"
    "v_max3_u32 v248, v248, v135, v136\n"
    "v_max3_u32 v248, v248, v137, v138\n"
    "v_max3_u32 v248, v248, v139, v140\n"
    "v_max3_u32 v248, v248, v141, v142\n"
    "v_max3_u32 v248, v248, v143, v144\n"
    "v_max3_u32 v248, v248, v145, v146\n"
    "v_max3_u32 v248, v248, v147, v148\n"
    "v_max3_u32 v248, v248, v149, v150\n"
    "v_max_u32 v248, v248, v151\n"
    "v_cmp_eq_u32 vcc, -1, v248\n"
    "s_waitcnt lgkmcnt(0)\n"
    "s_nop 3\n"
    "s_cbranch_vccz .Lp4_re\n"
    "s_add_u32 s79, s79, 1\n"
    "s_cmp_lt_u32 s79, 0x40000\n"
    "s_cbranch_scc1 .Lp4_slow_l\n"
    "s_branch .Lp4_re\n"
    ".Lp4_re:\n"
    "v_lshlrev_b32 v12, 16, v4\n"
    "v_and_b32 v13, 0xffff0000, v4\n"
    "v_lshlrev_b32 v14, 16, v5\n"
    "v_and_b32 v15, 0xffff0000, v5\n"
    "v_lshlrev_b32 v16, 16, v6\n"
    "v_and_b32 v17, 0xffff0000, v6\n"
    "v_lshlrev_b32 v18, 16, v7\n"
    "v_and_b32 v19, 0xffff0000, v7\n"
    "v_mfma_f32_16x16x32_bf16 v[12:15], v[160:163], v[64:67], v[12:15]\n"
    "s_nop 0\n"
    "v_mfma_f32_16x16x32_bf16 v[16:19], v[164:167], v[64:67], v[16:19]\n"
    "v_mfma_f32_16x16x32_bf16 v[20:23], v[168:171], v[64:67], 0\n"
    "v_mfma_f32_16x16x32_bf16 v[24:27], v[172:175], v[64:67], 0\n"
    "v_mfma_f32_16x16x32_bf16 v[12:15], v[176:179], v[68:71], v[12:15]\n"
    "v_mfma_f32_16x16x32_bf16 v[16:19], v[180:183], v[68:71], v[16:19]\n"
    "v_mfma_f32_16x16x32_bf16 v[20:23], v[184:187], v[68:71], v[20:23]\n"
    "v_mfma_f32_16x16x32_bf16 v[24:27], v[188:191], v[68:71], v[24:27]\n"
    "v_mfma_f32_16x16x32_bf16 v[12:15], v[192:195], v[72:75], v[12:15]\n"
    "v_mfma_f32_16x16x32_bf16 v[16:19], v[196:199], v[72:75], v[16:19]\n"
    "v_mfma_f32_16x16x32_bf16 v[20:23], v[200:203], v[72:75], v[20:23]\n"
    "v_mfma_f32_16x16x32_bf16 v[24:27], v[204:207], v[72:75], v[24:27]\n"
    "v_mfma_f32_16x16x32_bf16 v[12:15], v[208:211], v[76:79], v[12:15]\n"
    "v_mfma_f32_16x16x32_bf16 v[16:19], v[212:215], v[76:79], v[16:19]\n"
    "v_mfma_f32_16x16x32_bf16 v[20:23], v[216:219], v[76:79], v[20:23]\n"
    "ds_read_b128 v[44:47], v8 offset:20480\n"
    "v_mfma_f32_16x16x32_bf16 v[24:27], v[220:223], v[76:79], v[24:27]\n"
    "ds_read_b128 v[48:51], v8 offset:21504\n"
    "v_mfma_f32_16x16x32_bf16 v[12:15], v[224:227], v[80:83], v[12:15]\n"
    "ds_read_b128 v[52:55], v9 offset:20480\n"
    "v_mfma_f32_16x16x32_bf16 v[16:19], v[228:231], v[80:83], v[16:19]\n"
    "ds_read_b128 v[56:59], v9 offset:21504\n"
    "v_mfma_f32_16x16x32_bf16 v[20:23], v[232:235], v[80:83], v[20:23]\n"
    "ds_read_b128 v[28:31], v8 offset:24576\n"
    "v_mfma_f32_16x16x32_bf16 v[24:27], v[236:239], v[80:83], v[24:27]\n"
    "ds_read_b128 v[32:35], v8 offset:25600\n"
    "s_waitcnt lgkmcnt(5)\n"
    "v_mfma_f32_16x16x32_bf16 v[12:15], v[44:47], v[84:87], v[12:15]\n"
    "ds_read_b128 v[36:39], v9 offset:24576\n"
    "s_waitcnt lgkmcnt(5)\n"
    "v_mfma_f32_16x16x32_bf16 v[16:19], v[48:51], v[84:87], v[16:19]\n"
    "ds_read_b128 v[40:43], v9 offset:25600\n"
    "s_waitcnt lgkmcnt(5)\n"
    "v_mfma_f32_16x16x32_bf16 v[20:23], v[52:55], v[84:87], v[20:23]\n"
    "ds_read_b128 v[44:47], v8 offset:28672\n"
    "s_waitcnt lgkmcnt(5)\n"
    "v_mfma_f32_16x16x32_bf16 v[24:27], v[56:59], v[84:87], v[24:27]\n"
    "ds_read_b128 v[48:51], v8 offset:29696\n"
    "s_waitcnt lgkmcnt(5)\n"
    "v_mfma_f32_16x16x32_bf16 v[12:15], v[28:31], v[88:91], v[12:15]\n"
    "ds_read_b128 v[52:55], v9 offset:28672\n"
    "s_waitcnt lgkmcnt(5)\n"
    "v_mfma_f32_16x16x32_bf16 v[16:19], v[32:35], v[88:91], v[16:19]\n"
    "ds_read_b128 v[56:59], v9 offset:29696\n"
    "s_waitcnt lgkmcnt(5)\n"
    "v_mfma_f32_16x16x32_bf16 v[20:23], v[36:39], v[88:91], v[20:23]\n"
    "ds_read_b128 v[28:31], v8 offset:32768\n"
    "s_waitcnt lgkmcnt(5)\n"
    "v_mfma_f32_16x16x32_bf16 v[24:27], v[40:43], v[88:91], v[24:27]\n"
    "ds_read_b128 v[32:35], v8 offset:33792\n"
    "s_waitcnt lgkmcnt(5)\n"
    "v_mfma_f32_16x16x32_bf16 v[12:15], v[44:47], v[92:95], v[12:15]\n"
    "ds_read_b128 v[36:39], v9 offset:32768\n"
    "s_waitcnt lgkmcnt(5)\n"
    "v_mfma_f32_16x16x32_bf16 v[16:19], v[48:51], v[92:95], v[16:19]\n"
    "ds_read_b128 v[40:43], v9 offset:33792\n"
    "s_waitcnt lgkmcnt(5)\n"
    "v_mfma_f32_16x16x32_bf16 v[20:23], v[52:55], v[92:95], v[20:23]\n"
    "ds_read_b128 v[44:47], v60\n"
    "s_waitcnt lgkmcnt(5)\n"
    "v_mfma_f32_16x16x32_bf16 v[24:27], v[56:59], v[92:95], v[24:27]\n"
    "ds_read_b128 v[48:51], v60 offset:1024\n"
    "s_waitcnt lgkmcnt(5)\n"
    "v_mfma_f32_16x16x32_bf16 v[12:15], v[28:31], v[96:99], v[12:15]\n"
    "ds_read_b128 v[52:55], v61\n"
    "s_waitcnt lgkmcnt(5)\n"
    "v_mfma_f32_16x16x32_bf16 v[16:19], v[32:35], v[96:99], v[16:19]\n"
    "ds_read_b128 v[56:59], v61 offset:1024\n"
    "s_waitcnt lgkmcnt(5)\n"
    "v_mfma_f32_16x16x32_bf16 v[20:23], v[36:39], v[96:99], v[20:23]\n"
    "ds_read_b128 v[28:31], v60 offset:4096\n"
    "s_waitcnt lgkmcnt(5)\n"
    "v_mfma_f32_16x16x32_bf16 v[24:27], v[40:43], v[96:99], v[24:27]\n"
    "ds_read_b128 v[32:35], v60 offset:5120\n"
    "s_waitcnt lgkmcnt(5)\n"
    "v_mfma_f32_16x16x32_bf16 v[12:15], v[44:47], v[100:103], v[12:15]\n"
    "ds_read_b128 v[36:39], v61 offset:4096\n"
    "s_waitcnt lgkmcnt(5)\n"
    "v_mfma_f32_16x16x32_bf16 v[16:19], v[48:51], v[100:103], v[16:19]\n"
    "ds_read_b128 v[40:43], v61 offset:5120\n"
    "s_waitcnt lgkmcnt(5)\n"
    "v_mfma_f32_16x16x32_bf16 v[20:23], v[52:55], v[100:103], v[20:23]\n"
    "ds_read_b128 v[44:47], v60 offset:8192\n"
    "s_waitcnt lgkmcnt(5)\n"
    "v_mfma_f32_16x16x32_bf16 v[24:27], v[56:59], v[100:103], v[24:27]\n"
    "ds_read_b128 v[48:51], v60 offset:9216\n"
    "s_waitcnt lgkmcnt(5)\n"
    "v_mfma_f32_16x16x32_bf16 v[12:15], v[28:31], v[104:107], v[12:15]\n"
    "ds_read_b128 v[52:55], v61 offset:8192\n"
    "s_waitcnt lgkmcnt(5)\n"
    "v_mfma_f32_16x16x32_bf16 v[16:19], v[32:35], v[104:107], v[16:19]\n"
    "ds_read_b128 v[56:59], v61 offset:9216\n"
    "s_waitcnt lgkmcnt(5)\n"
    "v_mfma_f32_16x16x32_bf16 v[20:23], v[36:39], v[104:107], v[20:23]\n"
    "ds_read_b128 v[28:31], v60 offset:12288\n"
    "s_waitcnt lgkmcnt(5)\n"
    "v_mfma_f32_16x16x32_bf16 v[24:27], v[40:43], v[104:107], v[24:27]\n"
    "ds_read_b128 v[32:35], v60 offset:13312\n"
    "s_waitcnt lgkmcnt(5)\n"
    "v_mfma_f32_16x16x32_bf16 v[12:15], v[44:47], v[108:111], v[12:15]\n"
    "ds_read_b128 v[36:39], v61 offset:12288\n"
    "s_waitcnt lgkmcnt(5)\n"
    "v_mfma_f32_16x16x32_bf16 v[16:19], v[48:51], v[108:111], v[16:19]\n"
    "ds_read_b128 v[40:43], v61 offset:13312\n"
    "s_waitcnt lgkmcnt(5)\n"
    "v_mfma_f32_16x16x32_bf16 v[20:23], v[52:55], v[108:111], v[20:23]\n"
    "ds_read_b128 v[44:47], v60 offset:16384\n"
    "s_waitcnt lgkmcnt(5)\n"
    "v_mfma_f32_16x16x32_bf16 v[24:27], v[56:59], v[108:111], v[24:27]\n"
    "ds_read_b128 v[48:51], v60 offset:17408\n"
    "s_waitcnt lgkmcnt(5)\n"
    "v_mfma_f32_16x16x32_bf16 v[12:15], v[28:31], v[112:115], v[12:15]\n"
    "ds_read_b128 v[52:55], v61 offset:16384\n"
    "s_waitcnt lgkmcnt(5)\n"
    "v_mfma_f32_16x16x32_bf16 v[16:19], v[32:35], v[112:115], v[16:19]\n"
    "ds_read_b128 v[56:59], v61 offset:17408\n"
    "s_waitcnt lgkmcnt(5)\n"
    "v_mfma_f32_16x16x32_bf16 v[20:23], v[36:39], v[112:115], v[20:23]\n"
    "ds_read_b128 v[28:31], v60 offset:20480\n"
    "s_waitcnt lgkmcnt(5)\n"
    "v_mfma_f32_16x16x32_bf16 v[24:27], v[40:43], v[112:115], v[24:27]\n"
    "ds_read_b128 v[32:35], v60 offset:21504\n"
    "s_waitcnt lgkmcnt(5)\n"
    "v_mfma_f32_16x16x32_bf16 v[12:15], v[44:47], v[116:119], v[12:15]\n"
    "ds_read_b128 v[36:39], v61 offset:20480\n"
    "s_waitcnt lgkmcnt(5)\n"
    "v_mfma_f32_16x16x32_bf16 v[16:19], v[48:51], v[116:119], v[16:19]\n"
    "ds_read_b128 v[40:43], v61 offset:21504\n"
    "s_waitcnt lgkmcnt(5)\n"
    "v_mfma_f32_16x16x32_bf16 v[20:23], v[52:55], v[116:119], v[20:23]\n"
    "ds_read_b128 v[44:47], v60 offset:24576\n"
    "s_waitcnt lgkmcnt(5)\n"
    "v_mfma_f32_16x16x32_bf16 v[24:27], v[56:59], v[116:119], v[24:27]\n"
    "ds_read_b128 v[48:51], v60 offset:25600\n"
    "s_waitcnt lgkmcnt(5)\n"
    "v_mfma_f32_16x16x32_bf16 v[12:15], v[28:31], v[120:123], v[12:15]\n"
    "ds_read_b128 v[52:55], v61 offset:24576\n"
    "s_waitcnt lgkmcnt(5)\n"
    "v_mfma_f32_16x16x32_bf16 v[16:19], v[32:35], v[120:123], v[16:19]\n"
    "ds_read_b128 v[56:59], v61 offset:25600\n"
    "s_waitcnt lgkmcnt(5)\n"
    "v_mfma_f32_16x16x32_bf16 v[20:23], v[36:39], v[120:123], v[20:23]\n"
    "ds_read_b128 v[28:31], v60 offset:28672\n"
    "s_waitcnt lgkmcnt(5)\n"
    "v_mfma_f32_16x16x32_bf16 v[24:27], v[40:43], v[120:123], v[24:27]\n"
    "ds_read_b128 v[32:35], v60 offset:29696\n"
    "s_waitcnt lgkmcnt(5)\n"
    "v_mfma_f32_16x16x32_bf16 v[12:15], v[44:47], v[124:127], v[12:15]\n"
    "ds_read_b128 v[36:39], v61 offset:28672\n"
    "s_waitcnt lgkmcnt(5)\n"
    "v_mfma_f32_16x16x32_bf16 v[16:19], v[48:51], v[124:127], v[16:19]\n"
    "ds_read_b128 v[40:43], v61 offset:29696\n"
    "s_waitcnt lgkmcnt(5)\n"
    "v_mfma_f32_16x16x32_bf16 v[20:23], v[52:55], v[124:127], v[20:23]\n"
    "ds_read_b128 v[44:47], v60 offset:32768\n"
    "s_waitcnt lgkmcnt(5)\n"
    "v_mfma_f32_16x16x32_bf16 v[24:27], v[56:59], v[124:127], v[24:27]\n"
    "ds_read_b128 v[48:51], v60 offset:33792\n"
    "s_waitcnt lgkmcnt(5)\n"
    "v_mfma_f32_16x16x32_bf16 v[12:15], v[28:31], v[128:131], v[12:15]\n"
    "ds_read_b128 v[52:55], v61 offset:32768\n"
    "s_waitcnt lgkmcnt(5)\n"
    "v_mfma_f32_16x16x32_bf16 v[16:19], v[32:35], v[128:131], v[16:19]\n"
    "ds_read_b128 v[56:59], v61 offset:33792\n"
    "s_waitcnt lgkmcnt(5)\n"
    "v_mfma_f32_16x16x32_bf16 v[20:23], v[36:39], v[128:131], v[20:23]\n"
    "ds_read_b128 v[28:31], v60 offset:36864\n"
    "s_waitcnt lgkmcnt(5)\n"
    "v_mfma_f32_16x16x32_bf16 v[24:27], v[40:43], v[128:131], v[24:27]\n"
    "ds_read_b128 v[32:35], v60 offset:37888\n"
    "s_waitcnt lgkmcnt(5)\n"
    "v_mfma_f32_16x16x32_bf16 v[12:15], v[44:47], v[132:135], v[12:15]\n"
    "ds_read_b128 v[36:39], v61 offset:36864\n"
    "s_waitcnt lgkmcnt(5)\n"
    "v_mfma_f32_16x16x32_bf16 v[16:19], v[48:51], v[132:135], v[16:19]\n"
    "ds_read_b128 v[40:43], v61 offset:37888\n"
    "s_waitcnt lgkmcnt(5)\n"
    "v_mfma_f32_16x16x32_bf16 v[20:23], v[52:55], v[132:135], v[20:23]\n"
    "ds_read_b128 v[44:47], v8\n"
    "s_waitcnt lgkmcnt(5)\n"
    "v_mfma_f32_16x16x32_bf16 v[24:27], v[56:59], v[132:135], v[24:27]\n"
    "ds_read_b128 v[48:51], v8 offset:1024\n"
    "s_waitcnt lgkmcnt(5)\n"
    "v_mfma_f32_16x16x32_bf16 v[12:15], v[28:31], v[136:139], v[12:15]\n"
    "ds_read_b128 v[52:55], v9\n"
    "s_waitcnt lgkmcnt(5)\n"
    "v_mfma_f32_16x16x32_bf16 v[16:19], v[32:35], v[136:139], v[16:19]\n"
    "ds_read_b128 v[56:59], v9 offset:1024\n"
    "s_waitcnt lgkmcnt(5)\n"
    "v_mfma_f32_16x16x32_bf16 v[20:23], v[36:39], v[136:139], v[20:23]\n"
    "ds_read_b128 v[28:31], v8 offset:4096\n"
    "s_waitcnt lgkmcnt(5)\n"
    "v_mfma_f32_16x16x32_bf16 v[24:27], v[40:43], v[136:139], v[24:27]\n"
    "ds_read_b128 v[32:35], v8 offset:5120\n"
    "s_waitcnt lgkmcnt(5)\n"
    "v_mfma_f32_16x16x32_bf16 v[12:15], v[44:47], v[140:143], v[12:15]\n"
    "ds_read_b128 v[36:39], v9 offset:4096\n"
    "s_waitcnt lgkmcnt(5)\n"
    "v_mfma_f32_16x16x32_bf16 v[16:19], v[48:51], v[140:143], v[16:19]\n"
    "ds_read_b128 v[40:43], v9 offset:5120\n"
    "s_waitcnt lgkmcnt(5)\n"
    "v_mfma_f32_16x16x32_bf16 v[20:23], v[52:55], v[140:143], v[20:23]\n"
    "ds_read_b128 v[44:47], v8 offset:8192\n"
    "s_waitcnt lgkmcnt(5)\n"
    "v_mfma_f32_16x16x32_bf16 v[24:27], v[56:59], v[140:143], v[24:27]\n"
    "ds_read_b128 v[48:51], v8 offset:9216\n"
    "s_waitcnt lgkmcnt(5)\n"
    "v_mfma_f32_16x16x32_bf16 v[12:15], v[28:31], v[144:147], v[12:15]\n"
    "ds_read_b128 v[52:55], v9 offset:8192\n"
    "s_waitcnt lgkmcnt(5)\n"
    "v_mfma_f32_16x16x32_bf16 v[16:19], v[32:35], v[144:147], v[16:19]\n"
    "ds_read_b128 v[56:59], v9 offset:9216\n"
    "s_waitcnt lgkmcnt(5)\n"
    "v_mfma_f32_16x16x32_bf16 v[20:23], v[36:39], v[144:147], v[20:23]\n"
    "s_waitcnt lgkmcnt(4)\n"
    "v_mfma_f32_16x16x32_bf16 v[24:27], v[40:43], v[144:147], v[24:27]\n"
    "s_waitcnt lgkmcnt(3)\n"
    "v_mfma_f32_16x16x32_bf16 v[12:15], v[44:47], v[148:151], v[12:15]\n"
    "s_waitcnt lgkmcnt(2)\n"
    "v_mfma_f32_16x16x32_bf16 v[16:19], v[48:51], v[148:151], v[16:19]\n"
    "s_waitcnt lgkmcnt(1)\n"
    "v_mfma_f32_16x16x32_bf16 v[20:23], v[52:55], v[148:151], v[20:23]\n"
    "s_waitcnt lgkmcnt(0)\n"
    "v_mfma_f32_16x16x32_bf16 v[24:27], v[56:59], v[148:151], v[24:27]\n"
    "s_nop 5\n"
    "ds_write_b128 v10, v[20:23]\n"
    "s_nop 0\n"
    "ds_write_b128 v10, v[24:27] offset:1024\n"
    "s_nop 7\n"
    "s_branch .Lp4_cont\n"
    ".Lend:\n"
    : "+v"(tid) : "s"(__builtin_amdgcn_kernarg_segment_ptr()), "s"(blockIdx.x) : "v1", "v2", "v3", "v4", "v5", "v6", "v7", "v8", "v9", "v10", "v11", "v12", "v13", "v14", "v15", "v16", "v17", "v18", "v19", "v20", "v21", "v22", "v23", "v24", "v25", "v26", "v27", "v28", "v29", "v30", "v31", "v32", "v33", "v34", "v35", "v36", "v37", "v38", "v39", "v40", "v41", "v42", "v43", "v44", "v45", "v46", "v47", "v48", "v49", "v50", "v51", "v52", "v53", "v54", "v55", "v56", "v57", "v58", "v59", "v60", "v61", "v62", "v63", "v64", "v65", "v66", "v67", "v68", "v69", "v70", "v71", "v72", "v73", "v74", "v75", "v76", "v77", "v78", "v79", "v80", "v81", "v82", "v83", "v84", "v85", "v86", "v87", "v88", "v89", "v90", "v91", "v92", "v93", "v94", "v95", "v96", "v97", "v98", "v99", "v100", "v101", "v102", "v103", "v104", "v105", "v106", "v107", "v108", "v109", "v110", "v111", "v112", "v113", "v114", "v115", "v116", "v117", "v118", "v119", "v120", "v121", "v122", "v123", "v124", "v125", "v126", "v127", "v128", "v129", "v130", "v131", "v132", "v133", "v134", "v135", "v136", "v137", "v138", "v139", "v140", "v141", "v142", "v143", "v144", "v145", "v146", "v147", "v148", "v149", "v150", "v151", "v152", "v153", "v154", "v155", "v156", "v157", "v158", "v159", "v160", "v161", "v162", "v163", "v164", "v165", "v166", "v167", "v168", "v169", "v170", "v171", "v172", "v173", "v174", "v175", "v176", "v177", "v178", "v179", "v180", "v181", "v182", "v183", "v184", "v185", "v186", "v187", "v188", "v189", "v190", "v191", "v192", "v193", "v194", "v195", "v196", "v197", "v198", "v199", "v200", "v201", "v202", "v203", "v204", "v205", "v206", "v207", "v208", "v209", "v210", "v211", "v212", "v213", "v214", "v215", "v216", "v217", "v218", "v219", "v220", "v221", "v222", "v223", "v224", "v225", "v226", "v227", "v228", "v229", "v230", "v231", "v232", "v233", "v234", "v235", "v236", "v237", "v238", "v239", "v240", "v241", "v242", "v243", "v244", "v245", "v246", "v247", "v248", "v249", "v250", "v251", "v252", "v253", "v254", "v255", "s4", "s5", "s6", "s7", "s8", "s9", "s10", "s11", "s12", "s13", "s14", "s15", "s16", "s17", "s18", "s19", "s20", "s21", "s22", "s23", "s24", "s25", "s26", "s27", "s28", "s29", "s30", "s31", "s33", "s34", "s35", "s36", "s37", "s38", "s39", "s40", "s41", "s42", "s43", "s44", "s45", "s46", "s47", "s48", "s49", "s50", "s51", "s52", "s53", "s54", "s55", "s56", "s57", "s58", "s59", "s60", "s61", "s62", "s63", "s64", "s65", "s66", "s67", "s68", "s69", "s70", "s71", "s72", "s73", "s74", "s75", "s76", "s77", "s78", "s79", "s80", "s81", "s82", "s83", "s84", "s85", "s86", "s87", "s88", "s89", "s90", "s91", "s92", "s93", "s94", "s95", "s96", "s97", "s98", "s99", "vcc", "scc", "memory");
}
extern "C" void kernel_launch(void* const* d_in, const int* in_sizes, int n_in, void* d_out, int out_size, void* d_ws, size_t ws_size, hipStream_t stream) {
  Params p{d_in[0], d_in[1], d_in[2], d_in[4], d_in[5], d_in[6], d_out, d_ws};
  (void)hipFuncSetAttribute((const void*)fused_block, hipFuncAttributeMaxDynamicSharedMemorySize, 160 * 1024);
  fused_block<<<256, 512, 160 * 1024, stream>>>(p);
}
```
